```python
import torch

def _softmax(x, axis=-1):
    return torch.softmax(x, dim=axis)
def _gelu(x):
    return torch.nn.functional.gelu(x, approximate="tanh")
def _silu(x):
    return torch.nn.functional.silu(x)
def _sigmoid(x):
    return torch.sigmoid(x)
def _layernorm(x, axis=-1, eps=1e-5):
    m = x.mean(dim=axis, keepdim=True)
    v = x.var(dim=axis, keepdim=True, unbiased=False)
    return (x - m) / torch.sqrt(v + eps)
def _scatter_add(tgt, idx, src, axis=0):
    return tgt.clone().index_add_(axis, idx, src)
def _segment_sum(src, seg, n):
    out = torch.zeros((n,) + src.shape[1:], dtype=src.dtype)
    return out.index_add_(0, seg, src)
def _pad(x, pw):
    flat = tuple(v for pair in reversed(pw) for v in pair)
    return torch.nn.functional.pad(x, flat)

kernel_name = 'gated_mix__softmax_full_mha__glu_gated_glu__prompt'

def setup_inputs():

    _w = (lambda a: (lambda t: t.to(torch.bfloat16) if t.is_floating_point() else t)(torch.as_tensor(a)))
    def _A_setup():
        B, S, D = 1, 2048, 448
        return {
            "x": _w((lambda *s: torch.randn(*s, dtype=torch.bfloat16) * 0.05)(B, S, D)),
            "wqkv": _w((lambda *s: torch.randn(*s, dtype=torch.bfloat16) * 0.8)(D, 3 * D)),
            "wo": _w((lambda *s: torch.randn(*s, dtype=torch.bfloat16) * 0.1)(D, D)),
        }
    _A_in = _A_setup()

    def _B_setup():
        B, S, D, F = 1, 2048, 448, 1792
        return {
            "x": _w((lambda *s: torch.randn(*s, dtype=torch.bfloat16) * 0.05)(B, S, D)),
            "w_gate": _w((lambda *s: torch.randn(*s, dtype=torch.bfloat16) * 0.4)(D, F)),
            "w_up": _w((lambda *s: torch.randn(*s, dtype=torch.bfloat16) * 0.05)(D, F)),
            "w_down": _w((lambda *s: torch.randn(*s, dtype=torch.bfloat16) * 0.05)(F, D)),
        }
    _B_in = _B_setup()

    return {
        "A_x": _A_in["x"],
        "A_wqkv": _A_in["wqkv"],
        "A_wo": _A_in["wo"],
        "B_x": _B_in["x"],
        "B_w_gate": _B_in["w_gate"],
        "B_w_up": _B_in["w_up"],
        "B_w_down": _B_in["w_down"],
        "gate_w": _w((lambda *s: torch.randn(*s, dtype=torch.bfloat16) * 0.05)(448) * 16.0),
    }

def reference(A_x, A_wqkv, A_wo, B_x, B_w_gate, B_w_up, B_w_down, gate_w):
    def _A_ref(x, wqkv, wo):
        B, S, D = x.shape
        H = 16; Dh = D // H
        qkv = (lambda x, *d: x.permute(*d))((x @ wqkv).reshape(B, S, 3, H, Dh), 2, 0, 3, 1, 4)
        q, k, v = qkv[0], qkv[1], qkv[2]
        scale = Dh ** -0.5
        scores = torch.einsum("bhqd,bhkd->bhqk", q, k) * scale
        p = _softmax(scores, axis=-1)
        o = torch.einsum("bhqk,bhkd->bhqd", p, v)
        return (lambda x, *d: x.permute(*d))(o, 0, 2, 1, 3).reshape(B, S, D) @ wo

    def _B_ref(x, w_gate, w_up, w_down):
        g = torch.einsum("bsd,df->bsf", x, w_gate)
        u = torch.einsum("bsd,df->bsf", x, w_up)
        h = _sigmoid(g) * u
        return torch.einsum("bsf,fd->bsd", h, w_down)

    x = A_x
    g = _sigmoid((x * gate_w).sum(-1, keepdims=True))
    a = _A_ref(x, A_wqkv, A_wo)
    b = _B_ref(x, B_w_gate, B_w_up, B_w_down)
    return g * a + (1 - g) * b
```

```cpp
#include <hip/hip_runtime.h>
#include <hip/hip_bf16.h>
#include <stdint.h>

typedef __attribute__((ext_vector_type(8))) short bf16x8;
typedef __attribute__((ext_vector_type(4))) short s16x4;
typedef __attribute__((ext_vector_type(16))) float f32x16;
typedef __attribute__((ext_vector_type(4))) float f32x4;
typedef __attribute__((ext_vector_type(4))) unsigned u32x4;
typedef __attribute__((ext_vector_type(2))) unsigned u32x2;
typedef __attribute__((ext_vector_type(2))) float f32x2;
typedef __attribute__((ext_vector_type(2))) __bf16 bf16x2_t;
#define LAS __attribute__((address_space(3)))
typedef LAS const char* lds_cptr;
typedef LAS char* lds_ptr;

#define DI __device__ __forceinline__
#define MFMA32(a, b, c) __builtin_amdgcn_mfma_f32_32x32x16_bf16((a), (b), (c), 0, 0, 0)
#define MFMA16(a, b, c) __builtin_amdgcn_mfma_f32_16x16x32_bf16((a), (b), (c), 0, 0, 0)
#define SBAR() __builtin_amdgcn_sched_barrier(0)

constexpr int S_ = 2048, D_ = 448, H_ = 16, DH = 28, F_ = 1792;
constexpr float QSCALE = 0.18898223650461362f * 1.4426950408889634f;
constexpr float LOG2E = 1.4426950408889634f;

constexpr size_t WS_Q = 0;
constexpr size_t WS_KV = WS_Q + (2u << 20);
constexpr size_t WS_H = WS_KV + (4u << 20);
constexpr size_t WS_WD = WS_H + (7u << 20);
constexpr size_t WS_WO = WS_WD + (2u << 20);
constexpr size_t WS_O = WS_WO + (1u << 20);
constexpr size_t WS_G = WS_O + (2u << 20);

DI unsigned pk2(float lo, float hi) {
  f32x2 v = {lo, hi};
  bf16x2_t r = __builtin_convertvector(v, bf16x2_t);
  return __builtin_bit_cast(unsigned, r);
}
DI float bf2f(unsigned short u) { return __uint_as_float(((unsigned)u) << 16); }
DI float rbf(float x) { return __uint_as_float(pk2(x, 0.f) << 16); }

DI void glds16(const void* g, unsigned lds_base) {
  unsigned sv;
  asm volatile("s_mov_b32 %0, m0\n\ts_mov_b32 m0, %2\n\ts_nop 0\n\tglobal_load_lds_dwordx4 %1, off\n\ts_mov_b32 m0, %0"
               : "=&s"(sv) : "v"(g), "s"(lds_base) : "memory");
}
typedef short v4i16_t __attribute__((ext_vector_type(4)));
DI s16x4 trread(lds_cptr p) {
  return __builtin_bit_cast(s16x4, __builtin_amdgcn_ds_read_tr16_b64_v4i16((LAS v4i16_t*)p));
}
DI bf16x8 ldsread16(lds_cptr p) { return *(const LAS bf16x8*)p; }
DI bf16x8 cat8(s16x4 a, s16x4 b) { return __builtin_shufflevector(a, b, 0, 1, 2, 3, 4, 5, 6, 7); }
#define WAITV_BAR(N) do { SBAR(); asm volatile("s_waitcnt vmcnt(" #N ")\n\ts_barrier" ::: "memory"); } while (0)

constexpr int K1_XB = 8192, K1_WB = 20480, K1_STAGE = K1_XB + K1_WB, K1_NST = 5;
constexpr int K1_LDS = K1_STAGE * K1_NST + 64;

struct K1Frag { bf16x8 a; bf16x8 b[5]; };

template <bool VSET>
DI void k1_readfrag(K1Frag& f, lds_cptr xa, lds_cptr wb, int qoff) {
  f.a = ldsread16(xa);
  constexpr int TW = VSET ? 56 : 64;
#pragma unroll
  for (int t = 0; t < 4; ++t) f.b[t] = cat8(trread(wb + t * TW), trread(wb + t * TW + 4 * 320));
  f.b[4] = cat8(trread(wb + qoff), trread(wb + qoff + 4 * 320));
}
template <bool VSET>
DI void k1_mma(f32x16 (&acc)[5], const K1Frag& f) {
#pragma unroll
  for (int t = 0; t < 4; ++t) acc[t] = VSET ? MFMA32(f.a, f.b[t], acc[t]) : MFMA32(f.b[t], f.a, acc[t]);
  acc[4] = MFMA32(f.b[4], f.a, acc[4]);
}

template <bool VSET>
DI void k1_body(const char* __restrict__ x, const char* __restrict__ wqkv, const char* __restrict__ wg,
                const char* __restrict__ wu, char* __restrict__ ws, char* lds, int rb, int cb) {
  const int tid = threadIdx.x, lane = tid & 63, wid = __builtin_amdgcn_readfirstlane(tid >> 6);
  const int hh = lane >> 5, l31 = lane & 31;
  const unsigned lds0 = (unsigned)(uintptr_t)lds;
  const int row0 = rb * 128;
  const char* xsrc;
  {
    const int r = 16 * wid + (lane >> 2), c = (lane & 3) ^ ((r >> 2) & 3);
    xsrc = x + (size_t)(row0 + r) * (D_ * 2) + 16 * c;
  }
  const char* wsrc[3];
  int wstr[3];
  {
    const int cs = cb * 2 + (wid >> 2);
    const int win = 56 * cs - ((cs & 1) ? 8 : 0);
#pragma unroll
    for (int i = 0; i < 3; ++i) {
      int P = i * 64 + lane;
      if (P >= 160) P -= 32;
      const int r = 8 * (wid & 3) + P / 20, p = P % 20;
      if (p >= 16) { wsrc[i] = wqkv + (size_t)r * (3 * D_ * 2) + win + 16 * (p - 16); wstr[i] = 32 * 3 * D_ * 2; }
      else if (VSET) { const int pp = p < 14 ? p : 0; wsrc[i] = wqkv + (size_t)r * (3 * D_ * 2) + 1792 + 224 * (cs - 28) + 16 * pp; wstr[i] = 32 * 3 * D_ * 2; }
      else { wsrc[i] = (p < 8 ? wg : wu) + (size_t)r * (F_ * 2) + 128 * cs + 16 * (p & 7); wstr[i] = 32 * F_ * 2; }
    }
  }
  const unsigned xdst = lds0 + 1024 * wid, wdst = lds0 + K1_XB + 2560 * wid;
  auto stage = [&](int slot) {
    const unsigned sb = slot * K1_STAGE;
    glds16(xsrc, xdst + sb);
    glds16(wsrc[0], wdst + sb);
    glds16(wsrc[1], wdst + sb + 1024);
    if (lane < 32) glds16(wsrc[2], wdst + sb + 2048);
    xsrc += 64; wsrc[0] += wstr[0]; wsrc[1] += wstr[1]; wsrc[2] += wstr[2];
  };
  const int cs = cb * 2 + (wid >> 2);
  const int xr = 32 * (wid & 3) + l31;
  const lds_cptr L = (lds_cptr)(LAS char*)(uintptr_t)lds0;
  const lds_cptr xa0 = L + xr * 64 + 16 * ((0 + hh) ^ ((xr >> 2) & 3));
  const lds_cptr xa1 = L + xr * 64 + 16 * ((2 + hh) ^ ((xr >> 2) & 3));
  const lds_cptr wb0 = L + K1_XB + (wid >> 2) * 10240 + (8 * hh + ((lane & 15) >> 2)) * 320 + 32 * ((lane >> 4) & 1) + 8 * (lane & 3);
  const int qoff = 256 + ((cs & 1) ? 8 : 0);

  f32x16 acc[5];
#pragma unroll
  for (int t = 0; t < 5; ++t)
#pragma unroll
    for (int i = 0; i < 16; ++i) acc[t][i] = 0.f;

  stage(0); stage(1); stage(2); stage(3);
  WAITV_BAR(12);
  K1Frag F0, F1;
  k1_readfrag<VSET>(F0, xa0, wb0, qoff);
  int slot = 0;
#pragma unroll 1
  for (int c = 0; c < 14; ++c) {
    const int sb = slot * K1_STAGE;
    k1_readfrag<VSET>(F1, xa1 + sb, wb0 + sb + 16 * 320, qoff);
    k1_mma<VSET>(acc, F0);
    const int nslot = slot == K1_NST - 1 ? 0 : slot + 1;
    if (c < 13) {
      if (c <= 10) { WAITV_BAR(8); } else if (c == 11) { WAITV_BAR(4); } else { WAITV_BAR(0); }
      if (c + 4 < 14) stage(slot == 0 ? K1_NST - 1 : slot - 1);
      const int nb = nslot * K1_STAGE;
      k1_readfrag<VSET>(F0, xa0 + nb, wb0 + nb, qoff);
    }
    k1_mma<VSET>(acc, F1);
    slot = nslot;
  }

  const int rt32 = rb * 4 + (wid & 3);
  {
    const int t = cs;
    f32x16 v = acc[4];
    if (t < 16) {
#pragma unroll
      for (int i = 0; i < 16; ++i) v[i] *= QSCALE;
      if (hh) { v[12] = 0.f; v[13] = 0.f; v[14] = 0.f; v[15] = 0.f; }
    } else {
      if (hh) { v[12] = 1.f; v[13] = 0.f; v[14] = 0.f; v[15] = 0.f; }
    }
    char* dst = t < 16 ? ws + WS_Q + ((size_t)(t * 64 + rt32) * 2) * 1024 : ws + WS_KV + ((size_t)((t - 16) * 64 + rt32) * 4) * 1024;
#pragma unroll
    for (int s = 0; s < 2; ++s) {
      u32x4 w = {pk2(v[8 * s], v[8 * s + 1]), pk2(v[8 * s + 2], v[8 * s + 3]), pk2(v[8 * s + 4], v[8 * s + 5]), pk2(v[8 * s + 6], v[8 * s + 7])};
      *(u32x4*)(dst + s * 1024 + lane * 16) = w;
    }
  }
  if (VSET) {
#pragma unroll
    for (int t = 0; t < 4; ++t) {
      const int head = 4 * (cs - 28) + t;
      f32x16 v = acc[t];
      if (l31 >= 28) {
        const float f = l31 == 28 ? 1.f : 0.f;
#pragma unroll
        for (int i = 0; i < 16; ++i) v[i] = f;
      }
      char* dst = ws + WS_KV + ((size_t)(head * 64 + rt32) * 4 + 2) * 1024;
#pragma unroll
      for (int s = 0; s < 2; ++s) {
        u32x4 w = {pk2(v[8 * s], v[8 * s + 1]), pk2(v[8 * s + 2], v[8 * s + 3]), pk2(v[8 * s + 4], v[8 * s + 5]), pk2(v[8 * s + 6], v[8 * s + 7])};
        *(u32x4*)(dst + s * 1024 + lane * 16) = w;
      }
    }
  } else {
#pragma unroll
    for (int p = 0; p < 2; ++p) {
      float hv[16];
#pragma unroll
      for (int i = 0; i < 16; ++i) {
        const float e = __builtin_amdgcn_exp2f(-LOG2E * acc[p][i]);
        hv[i] = acc[p + 2][i] * __builtin_amdgcn_rcpf(1.f + e);
      }
      const int m16 = rt32 * 2 + (l31 >> 4), ks = 2 * cs + p;
      char* dst = ws + WS_H + ((size_t)(m16 * 56 + ks)) * 1024 + ((lane & 15) + 16 * hh) * 16;
#pragma unroll
      for (int s = 0; s < 2; ++s) {
        u32x4 w = {pk2(hv[8 * s], hv[8 * s + 1]), pk2(hv[8 * s + 2], hv[8 * s + 3]), pk2(hv[8 * s + 4], hv[8 * s + 5]), pk2(hv[8 * s + 6], hv[8 * s + 7])};
        *(u32x4*)(dst + s * 512) = w;
      }
    }
  }
}

__global__ __launch_bounds__(512, 2) void k1_kernel(const char* __restrict__ x, const char* __restrict__ wqkv, const char* __restrict__ wo,
                                                     const char* __restrict__ wg, const char* __restrict__ wu, const char* __restrict__ wd,
                                                     const char* __restrict__ gw, char* __restrict__ ws) {
  extern __shared__ __attribute__((aligned(16))) char lds[];
  const int bid = blockIdx.x, xcd = bid & 7, idx = bid >> 3;
  const int rb = 8 * (xcd & 1) + (idx & 7), cb = 4 * (xcd >> 1) + (idx >> 3);
  const int lane = threadIdx.x & 63, wid = __builtin_amdgcn_readfirstlane(threadIdx.x >> 6);
  const int fid = bid * 8 + wid;
  unsigned short rt[8];
  const int g4 = lane >> 4;
  char* rdst = nullptr;
  if (fid < 2016) {
    const bool isd = fid < 1568;
    const int f2 = isd ? fid : fid - 1568;
    const int nt = isd ? f2 / 56 : f2 / 16, ks = isd ? f2 % 56 : f2 % 16;
    const unsigned short* src = (const unsigned short*)(isd ? wd : wo);
    const int kbase = isd ? 32 * ks : 28 * ks;
#pragma unroll
    for (int j = 0; j < 8; ++j) {
      const int kk = 16 * (g4 >> 1) + 8 * (j >> 2) + 4 * (g4 & 1) + (j & 3);
      const bool ok = isd || kk < 28;
      rt[j] = src[(size_t)(kbase + (ok ? kk : 0)) * D_ + 16 * nt + (lane & 15)];
    }
    rdst = ws + (isd ? WS_WD : WS_WO) + (size_t)f2 * 1024 + lane * 16;
  }
  u32x4 gx = {0, 0, 0, 0}, gwv = {0, 0, 0, 0};
  if (lane < 56) {
    gx = *(const u32x4*)(x + (size_t)fid * (D_ * 2) + lane * 16);
    gwv = *(const u32x4*)(gw + lane * 16);
  }

  if (cb < 14) k1_body<false>(x, wqkv, wg, wu, ws, lds, rb, cb);
  else k1_body<true>(x, wqkv, wg, wu, ws, lds, rb, cb);

  if (fid < 2016) {
    if (fid >= 1568 && g4 == 3) { rt[4] = 0; rt[5] = 0; rt[6] = 0; rt[7] = 0; }
    u32x4 w = {(unsigned)rt[0] | ((unsigned)rt[1] << 16), (unsigned)rt[2] | ((unsigned)rt[3] << 16),
               (unsigned)rt[4] | ((unsigned)rt[5] << 16), (unsigned)rt[6] | ((unsigned)rt[7] << 16)};
    *(u32x4*)rdst = w;
  }
  {
    float s = 0.f;
#pragma unroll
    for (int i = 0; i < 4; ++i) {
      s += rbf(__uint_as_float(gx[i] << 16) * __uint_as_float(gwv[i] << 16));
      s += rbf(__uint_as_float(gx[i] & 0xffff0000u) * __uint_as_float(gwv[i] & 0xffff0000u));
    }
#pragma unroll
    for (int o = 32; o >= 1; o >>= 1) s += __shfl_xor(s, o);
    if (lane == 0) {
      const float sb = rbf(s);
      const float g = 1.f / (1.f + __expf(-sb));
      ((float*)(ws + WS_G))[fid] = rbf(g);
    }
  }
}

constexpr int K2_STAGE = 32768, K2_NST = 4;
constexpr int K2_LDS = K2_STAGE * K2_NST;

DI float xhalf_max(float v) {
  auto r = __builtin_amdgcn_permlane32_swap(__float_as_uint(v), __float_as_uint(v), false, false);
  return fmaxf(__uint_as_float(r[0]), __uint_as_float(r[1]));
}
DI float max16(const f32x16& s) {
  float a = fmaxf(fmaxf(s[0], s[1]), s[2]), b = fmaxf(fmaxf(s[3], s[4]), s[5]);
  a = fmaxf(fmaxf(a, s[6]), s[7]); b = fmaxf(fmaxf(b, s[8]), s[9]);
  a = fmaxf(fmaxf(a, s[10]), s[11]); b = fmaxf(fmaxf(b, s[12]), s[13]);
  a = fmaxf(fmaxf(a, s[14]), s[15]);
  return fmaxf(a, b);
}
DI void exp_pack(const f32x16& s, bf16x8& p0, bf16x8& p1) {
  float e[16];
#pragma unroll
  for (int i = 0; i < 16; ++i) e[i] = __builtin_amdgcn_exp2f(s[i]);
  u32x4 w0 = {pk2(e[0], e[1]), pk2(e[2], e[3]), pk2(e[4], e[5]), pk2(e[6], e[7])};
  u32x4 w1 = {pk2(e[8], e[9]), pk2(e[10], e[11]), pk2(e[12], e[13]), pk2(e[14], e[15])};
  p0 = __builtin_bit_cast(bf16x8, w0); p1 = __builtin_bit_cast(bf16x8, w1);
}

__global__ __launch_bounds__(512, 2) void k2_kernel(char* __restrict__ ws) {
  extern __shared__ __attribute__((aligned(16))) char lds[];
  const int bid = blockIdx.x, xcd = bid & 7, idx = bid >> 3;
  const int head = 2 * xcd + (idx & 1), qt = idx >> 1;
  const int tid = threadIdx.x, lane = tid & 63, wid = __builtin_amdgcn_readfirstlane(tid >> 6);
  const int hh = lane >> 5, qs = wid & 3, kh = wid >> 2;
  const unsigned lds0 = (unsigned)(uintptr_t)lds;
  const lds_cptr L = (lds_cptr)(LAS char*)(uintptr_t)lds0;
  const char* kvh = ws + WS_KV + (size_t)head * (64 * 4096);
  const char* ksrc = kvh + (size_t)(kh * 32 + qs) * 4096 + lane * 16;
  const unsigned kdst = lds0 + wid * 4096;
  auto stage = [&](int slot) {
    const unsigned d = kdst + slot * K2_STAGE;
    glds16(ksrc, d); glds16(ksrc + 1024, d + 1024); glds16(ksrc + 2048, d + 2048); glds16(ksrc + 3072, d + 3072);
    ksrc += 4 * 4096;
  };
  stage(0);
  const char* qsrc = ws + WS_Q + ((size_t)(head * 64 + qt * 4 + qs) * 2) * 1024 + lane * 16;
  bf16x8 q0 = *(const bf16x8*)qsrc, q1 = *(const bf16x8*)(qsrc + 1024);
  stage(1); stage(2);
  WAITV_BAR(8);
  const lds_cptr kb = L + kh * 16384 + lane * 16;
  f32x16 zero16;
#pragma unroll
  for (int i = 0; i < 16; ++i) zero16[i] = 0.f;
  float mref;
  {
    f32x16 s = MFMA32(ldsread16(kb), q0, zero16);
    s = MFMA32(ldsread16(kb + 1024), q1, s);
    mref = rbf(xhalf_max(max16(s)));
  }
  bf16x8 q1m = q1;
  if (hh) q1m[4] = (short)((__float_as_uint(-mref)) >> 16);
  f32x16 o = zero16;
  f32x16 sA = MFMA32(ldsread16(kb), q0, zero16);
  sA = MFMA32(ldsread16(kb + 1024), q1m, sA);
  f32x16 sB;
#pragma unroll 1
  for (int st = 0; st < 8; ++st) {
    const lds_cptr b = kb + (st & 3) * K2_STAGE;
    bf16x8 p0, p1;
    sB = MFMA32(ldsread16(b + 4096), q0, zero16); sB = MFMA32(ldsread16(b + 4096 + 1024), q1m, sB);
    exp_pack(sA, p0, p1);
    o = MFMA32(ldsread16(b + 2048), p0, o); o = MFMA32(ldsread16(b + 3072), p1, o);
    sA = MFMA32(ldsread16(b + 8192), q0, zero16); sA = MFMA32(ldsread16(b + 8192 + 1024), q1m, sA);
    exp_pack(sB, p0, p1);
    o = MFMA32(ldsread16(b + 4096 + 2048), p0, o); o = MFMA32(ldsread16(b + 4096 + 3072), p1, o);
    sB = MFMA32(ldsread16(b + 12288), q0, zero16); sB = MFMA32(ldsread16(b + 12288 + 1024), q1m, sB);
    exp_pack(sA, p0, p1);
    o = MFMA32(ldsread16(b + 8192 + 2048), p0, o); o = MFMA32(ldsread16(b + 8192 + 3072), p1, o);
    if (st < 7) {
      if (st < 6) { WAITV_BAR(4); } else { WAITV_BAR(0); }
      if (st + 3 < 8) stage((st + 3) & 3);
      const lds_cptr nb = kb + ((st + 1) & 3) * K2_STAGE;
      sA = MFMA32(ldsread16(nb), q0, zero16); sA = MFMA32(ldsread16(nb + 1024), q1m, sA);
    }
    exp_pack(sB, p0, p1);
    o = MFMA32(ldsread16(b + 12288 + 2048), p0, o); o = MFMA32(ldsread16(b + 12288 + 3072), p1, o);
  }
  {
    float mx = 0.f;
    bool bad = false;
#pragma unroll
    for (int i = 0; i < 16; ++i) { const float a = fabsf(o[i]); bad |= !(a <= 1.0e30f); mx = fmaxf(mx, a); }
    if (__any(bad)) {
      const char* kv = kvh + (size_t)(kh * 32) * 4096 + lane * 16;
      float m = -INFINITY;
      o = zero16;
#pragma unroll 1
      for (int t = 0; t < 32; ++t) {
        const char* p = kv + (size_t)t * 4096;
        f32x16 s = MFMA32(*(const bf16x8*)p, q0, zero16);
        s = MFMA32(*(const bf16x8*)(p + 1024), q1, s);
        const float mn = fmaxf(m, xhalf_max(max16(s)));
        const float al = __builtin_amdgcn_exp2f(m - mn);
        m = mn;
#pragma unroll
        for (int i = 0; i < 16; ++i) { o[i] *= al; s[i] -= mn; }
        bf16x8 p0, p1;
        exp_pack(s, p0, p1);
        o = MFMA32(*(const bf16x8*)(p + 2048), p0, o); o = MFMA32(*(const bf16x8*)(p + 3072), p1, o);
      }
      mref = m;
    }
  }
  __syncthreads();
  float* xb = (float*)lds + qs * (17 * 64);
  if (kh == 1) {
#pragma unroll
    for (int i = 0; i < 16; ++i) xb[i * 64 + lane] = o[i];
    xb[16 * 64 + lane] = mref;
  }
  __syncthreads();
  if (kh == 0) {
    const float m1 = xb[16 * 64 + lane];
    const float M = fmaxf(mref, m1);
    const float a0 = __builtin_amdgcn_exp2f(mref - M), a1 = __builtin_amdgcn_exp2f(m1 - M);
#pragma unroll
    for (int i = 0; i < 16; ++i) o[i] = o[i] * a0 + xb[i * 64 + lane] * a1;
    auto r = __builtin_amdgcn_permlane32_swap(__float_as_uint(o[12]), __float_as_uint(o[12]), false, false);
    const float lsum = __uint_as_float(r[1]);
    const float inv = 1.f / lsum;
    const int l31 = lane & 31;
    const int m16 = (qt * 4 + qs) * 2 + (l31 >> 4);
    char* dst = ws + WS_O + ((size_t)(m16 * 16 + head)) * 1024 + ((lane & 15) + 16 * hh) * 16;
#pragma unroll
    for (int s = 0; s < 2; ++s) {
      u32x4 w = {pk2(o[8 * s] * inv, o[8 * s + 1] * inv), pk2(o[8 * s + 2] * inv, o[8 * s + 3] * inv),
                 pk2(o[8 * s + 4] * inv, o[8 * s + 5] * inv), pk2(o[8 * s + 6] * inv, o[8 * s + 7] * inv)};
      *(u32x4*)(dst + s * 512) = w;
    }
  }
}

constexpr int K3_LDS = 8 * 14 * 1024;
__global__ __launch_bounds__(512, 2) void k3_kernel(const char* __restrict__ ws, unsigned short* __restrict__ out) {
  extern __shared__ __attribute__((aligned(16))) char lds[];
  const int bid = blockIdx.x, xcd = bid & 7, idx = bid >> 3;
  const int ct = xcd & 3, rt = (xcd >> 2) * 32 + idx;
  const int tid = threadIdx.x, lane = tid & 63, wid = __builtin_amdgcn_readfirstlane(tid >> 6);
  int k0, k1, nks;
  const char *asrc, *bsrc;
  if (wid < 2) { k0 = 8 * wid; k1 = k0 + 8; nks = 16; asrc = ws + WS_WO; bsrc = ws + WS_O; }
  else { const int w = wid - 2; k0 = w < 2 ? 10 * w : 20 + 9 * (w - 2); k1 = k0 + (w < 2 ? 10 : 9); nks = 56; asrc = ws + WS_WD; bsrc = ws + WS_H; }
  asrc += (size_t)(ct * 7) * nks * 1024 + lane * 16;
  bsrc += (size_t)(rt * 2) * nks * 1024 + lane * 16;
  f32x4 acc[7][2];
#pragma unroll
  for (int n = 0; n < 7; ++n) { acc[n][0] = f32x4{0, 0, 0, 0}; acc[n][1] = f32x4{0, 0, 0, 0}; }
#pragma unroll 2
  for (int k = k0; k < k1; ++k) {
    bf16x8 a[7], b[2];
#pragma unroll
    for (int n = 0; n < 7; ++n) a[n] = *(const bf16x8*)(asrc + (size_t)(n * nks + k) * 1024);
    b[0] = *(const bf16x8*)(bsrc + (size_t)k * 1024);
    b[1] = *(const bf16x8*)(bsrc + (size_t)(nks + k) * 1024);
#pragma unroll
    for (int n = 0; n < 7; ++n) { acc[n][0] = MFMA16(a[n], b[0], acc[n][0]); acc[n][1] = MFMA16(a[n], b[1], acc[n][1]); }
  }
  f32x4* red = (f32x4*)lds;
#pragma unroll
  for (int n = 0; n < 7; ++n) { red[(wid * 14 + n * 2 + 0) * 64 + lane] = acc[n][0]; red[(wid * 14 + n * 2 + 1) * 64 + lane] = acc[n][1]; }
  __syncthreads();
  for (int t = wid; t < 14; t += 8) {
    f32x4 a = red[(0 * 14 + t) * 64 + lane] + red[(1 * 14 + t) * 64 + lane];
    f32x4 b = red[(2 * 14 + t) * 64 + lane];
#pragma unroll
    for (int w = 3; w < 8; ++w) b += red[(w * 14 + t) * 64 + lane];
    const int n = t >> 1, mi = t & 1;
    const int row = rt * 32 + mi * 16 + (lane & 15), col = ct * 112 + n * 16 + 4 * (lane >> 4);
    const float g = ((const float*)(ws + WS_G))[row];
    const float g1 = rbf(1.f - g);
    float r[4];
#pragma unroll
    for (int i = 0; i < 4; ++i) r[i] = rbf(g * rbf(a[i])) + rbf(g1 * rbf(b[i]));
    u32x2 w = {pk2(r[0], r[1]), pk2(r[2], r[3])};
    *(u32x2*)(out + (size_t)row * D_ + col) = w;
  }
}

extern "C" void kernel_launch(void* const* d_in, const int* in_sizes, int n_in, void* d_out, int out_size, void* d_ws, size_t ws_size,
                              hipStream_t stream) {
  const char* x = (const char*)d_in[0];
  const char* wqkv = (const char*)d_in[1];
  const char* wo = (const char*)d_in[2];
  const char* wg = (const char*)d_in[4];
  const char* wu = (const char*)d_in[5];
  const char* wd = (const char*)d_in[6];
  const char* gw = (const char*)d_in[7];
  char* ws = (char*)d_ws;
  (void)hipFuncSetAttribute((const void*)k1_kernel, hipFuncAttributeMaxDynamicSharedMemorySize, K1_LDS);
  (void)hipFuncSetAttribute((const void*)k2_kernel, hipFuncAttributeMaxDynamicSharedMemorySize, K2_LDS);
  (void)hipFuncSetAttribute((const void*)k3_kernel, hipFuncAttributeMaxDynamicSharedMemorySize, K3_LDS);
  hipLaunchKernelGGL(k1_kernel, dim3(256), dim3(512), K1_LDS, stream, x, wqkv, wo, wg, wu, wd, gw, ws);
  hipLaunchKernelGGL(k2_kernel, dim3(256), dim3(512), K2_LDS, stream, ws);
  hipLaunchKernelGGL(k3_kernel, dim3(256), dim3(512), K3_LDS, stream, (const char*)ws, (unsigned short*)d_out);
}
```

```cpp
#include <hip/hip_runtime.h>
#include <hip/hip_bf16.h>
#include <stdint.h>

typedef __attribute__((ext_vector_type(8))) short bf16x8;
typedef __attribute__((ext_vector_type(4))) short s16x4;
typedef __attribute__((ext_vector_type(16))) float f32x16;
typedef __attribute__((ext_vector_type(4))) float f32x4;
typedef __attribute__((ext_vector_type(4))) unsigned u32x4;
typedef __attribute__((ext_vector_type(2))) unsigned u32x2;
typedef __attribute__((ext_vector_type(2))) float f32x2;
typedef __attribute__((ext_vector_type(2))) __bf16 bf16x2_t;
#define LAS __attribute__((address_space(3)))
#define GAS __attribute__((address_space(1)))
typedef LAS const char* lds_cptr;
typedef LAS char* lds_ptr;
typedef __amdgpu_buffer_rsrc_t rsrc_t;

#define DI __device__ __forceinline__
#define MFMA32(a, b, c) __builtin_amdgcn_mfma_f32_32x32x16_bf16((a), (b), (c), 0, 0, 0)
#define SBAR() __builtin_amdgcn_sched_barrier(0)
#define PIN(x) asm volatile("" : "+v"(x))
#define EX2(x) __builtin_amdgcn_exp2f(x)

#define STAMP_AT(ws, slot) do {} while (0)

constexpr int D_ = 448, F_ = 1792;
constexpr int XROW = D_ * 2, QROW = 3 * D_ * 2, FROW = F_ * 2;
constexpr float QSCALE = 0.18898223650461362f * 1.4426950408889634f;
constexpr float LOG2E = 1.4426950408889634f;

constexpr unsigned WS_Q = 0;
constexpr unsigned WS_KV = WS_Q + (2u << 20);
constexpr unsigned WS_H = WS_KV + (4u << 20);
constexpr unsigned WS_O = WS_H + (7u << 20);
constexpr unsigned WS_G = WS_O + (2u << 20);
constexpr unsigned WS_F = WS_G + 8192;
constexpr unsigned F_GEN = WS_F, F_QKV = WS_F + 1024, F_H = WS_F + 4096, F_O = WS_F + 8192;
constexpr unsigned WS_END = WS_F + 16384;

template <int I> struct IC { static constexpr int v = I; };
template <int B, int E, class Fn> DI void sfor(Fn&& f) {
  if constexpr (B < E) { f(IC<B>{}); sfor<B + 1, E>(f); }
}

DI unsigned pk2(float lo, float hi) {
  f32x2 v = {lo, hi};
  bf16x2_t r = __builtin_convertvector(v, bf16x2_t);
  return __builtin_bit_cast(unsigned, r);
}
DI float rbf(float x) { return __uint_as_float(pk2(x, 0.f) << 16); }
DI u32x4 pack8(const float* v) { return u32x4{pk2(v[0], v[1]), pk2(v[2], v[3]), pk2(v[4], v[5]), pk2(v[6], v[7])}; }

DI void glds16(const char* base, unsigned voff, unsigned lds_base) {
  asm volatile("s_mov_b32 m0, %2\n\ts_nop 0\n\tglobal_load_lds_dwordx4 %0, %1" ::"v"(voff), "s"(base), "s"(lds_base) : "memory");
}
DI void glds16_dev(const char* base, unsigned voff, unsigned lds_base) {
  asm volatile("s_mov_b32 m0, %2\n\ts_nop 0\n\tglobal_load_lds_dwordx4 %0, %1 sc1" ::"v"(voff), "s"(base), "s"(lds_base) : "memory");
}
typedef short v4i16_t __attribute__((ext_vector_type(4)));
DI s16x4 trread(lds_cptr p) {
  return __builtin_bit_cast(s16x4, __builtin_amdgcn_ds_read_tr16_b64_v4i16((LAS v4i16_t*)p));
}
DI bf16x8 ldsread16(lds_cptr p) { return *(const LAS bf16x8*)p; }
DI bf16x8 cat8(s16x4 a, s16x4 b) { return __builtin_shufflevector(a, b, 0, 1, 2, 3, 4, 5, 6, 7); }
template <int N> DI void waitv_bar() {
  SBAR();
  asm volatile("s_waitcnt vmcnt(%0) lgkmcnt(0)\n\ts_barrier" ::"i"(N) : "memory");
  SBAR();
}
DI void st16(rsrc_t r, unsigned off, u32x4 v) { __builtin_amdgcn_raw_buffer_store_b128(v, r, off, 0, 16); }
DI void st4(rsrc_t r, unsigned off, unsigned v) { __builtin_amdgcn_raw_buffer_store_b32(v, r, off, 0, 16); }
DI u32x4 ld16(rsrc_t r, unsigned off) { return __builtin_amdgcn_raw_buffer_load_b128(r, off, 0, 16); }
DI unsigned ld4(rsrc_t r, unsigned off) { return __builtin_amdgcn_raw_buffer_load_b32(r, off, 0, 16); }
DI bf16x8 ldfrag(rsrc_t r, unsigned off) { return __builtin_bit_cast(bf16x8, ld16(r, off)); }

struct Args {
  const char *x, *wqkv, *wo, *wg, *wu, *wd, *gw;
  char* ws;
  char* out;
};

constexpr int LDS_BYTES = 163840;
constexpr int SLOT = 40960;
DI constexpr int pos1(int s) { return ((s + 1) & 3) * SLOT; }

constexpr int SPIN_CAP = 100000;
DI void wait_flags(rsrc_t rs, unsigned off, int n, unsigned epoch, int lane) {
  const unsigned o = off + 4 * (lane < n ? lane : 0);
  for (int spin = 0; spin < SPIN_CAP; ++spin) {
    const unsigned v = ld4(rs, o);
    if (__all(v == epoch)) break;
    __builtin_amdgcn_s_sleep(2);
  }
  asm volatile("" ::: "memory");
}
DI void wait_flags256(rsrc_t rs, unsigned off, unsigned epoch, int lane) {
  for (int spin = 0; spin < SPIN_CAP; ++spin) {
    const u32x4 v = ld16(rs, off + 16 * lane);
    if (__all(v[0] == epoch && v[1] == epoch && v[2] == epoch && v[3] == epoch)) break;
    __builtin_amdgcn_s_sleep(2);
  }
  asm volatile("" ::: "memory");
}

DI float sigm_mul(float g, float u) { return u * __builtin_amdgcn_rcpf(1.f + EX2(-LOG2E * g)); }

template <bool VB>
DI unsigned phase1(const Args& A, rsrc_t rs, char* lds, int rb, int cgp) {
  const int tid = threadIdx.x, lane = tid & 63, wid = __builtin_amdgcn_readfirstlane(tid >> 6);
  const int hh = lane >> 5, l31 = lane & 31, rsb = wid & 3, cg = wid >> 2;
  const int q4 = (lane & 15) >> 2, p4 = lane & 3, blk = (lane >> 4) & 1;
  const unsigned lds0 = (unsigned)(uintptr_t)lds;
  const lds_cptr L = (lds_cptr)(LAS char*)(uintptr_t)lds0;
  const int r8 = lane >> 3, c8 = lane & 7;
  const int xr0 = 16 * wid + r8;
  const char* xs = A.x + (size_t)(128 * rb + 16 * wid) * XROW;
  const unsigned xc0 = r8 * XROW + 16 * (c8 ^ ((xr0 >> 1) & 7)), xc1 = (r8 + 8) * XROW + 16 * (c8 ^ (((xr0 + 8) >> 1) & 7));
  const int sw0 = 32 * ((c8 >> 1) ^ (r8 & 3)) + 16 * (c8 & 1), sw1 = sw0 ^ 32;
  constexpr int WROW = VB ? QROW : FROW;
  const char *wa0, *wa1, *wa2, *wbs;
  const unsigned oq = r8 * QROW + sw0, og = r8 * WROW + sw0, ou = r8 * WROW + (VB ? sw0 : sw1);
  const unsigned ob = (wid & 1) ? ou : og;
  if constexpr (VB) {
    const int l0 = cgp == 14 ? 14 : 18;
    wa0 = A.wqkv + (size_t)(8 * wid) * QROW + 128 * l0;
    wa1 = wa0 + 128; wa2 = wa0 + 256;
    wbs = A.wqkv + 128 * 17;
  } else {
    wa0 = A.wqkv + (size_t)(8 * wid) * QROW + 128 * cgp;
    wa1 = A.wg + (size_t)(8 * wid) * FROW + 256 * cgp;
    wa2 = A.wu + (size_t)(8 * wid) * FROW + 256 * cgp;
    wbs = ((wid & 1) ? A.wu : A.wg) + 256 * cgp + 128;
  }
  auto piece = [&](auto S, auto P) {
    constexpr int s = decltype(S)::v, p = decltype(P)::v;
    if constexpr (s < 7) {
      const unsigned sb = lds0 + pos1(s);
      if constexpr (p == 0) glds16(xs + 128 * s, xc0, sb + 2048 * wid);
      if constexpr (p == 1) glds16(xs + 128 * s, xc1, sb + 2048 * wid + 1024);
      if constexpr (p == 2) glds16(wa0 + (size_t)(64 * s) * QROW, oq, sb + 16384 + 1024 * wid);
      if constexpr (p == 3) glds16(wa1 + (size_t)(64 * s) * WROW, og, sb + 24576 + 1024 * wid);
      if constexpr (p == 4) glds16(wa2 + (size_t)(64 * s) * WROW, ou, sb + 32768 + 1024 * wid);
    } else if constexpr (s < 10) {
      constexpr int j = s - 7, n = j < 2 ? 5 : 4;
      if constexpr (p < n) {
        const int g = 20 * j + n * (wid >> 1) + p;
        glds16(wbs + (size_t)(8 * g) * WROW, ob, lds0 + g * 2048 + (wid & 1) * 1024);
      }
    }
  };
  const int grow = 128 * rb + 8 * cgp + wid;
  unsigned genv; u32x4 gx, gwv;
  {
    const int gl = (lane < 56 ? lane : 55) * 16;
    asm volatile("global_load_dword %0, %1, off sc1" : "=v"(genv) : "v"(A.ws + F_GEN) : "memory");
    asm volatile("global_load_dwordx4 %0, %1, off" : "=v"(gx) : "v"(A.x + (size_t)grow * XROW + gl) : "memory");
    asm volatile("global_load_dwordx4 %0, %1, off" : "=v"(gwv) : "v"(A.gw + gl) : "memory");
  }
  sfor<0, 4>([&](auto S) {
    sfor<0, 5>([&](auto P) { piece(S, P); });
    if constexpr (decltype(S)::v < 3) asm volatile("s_barrier" ::: "memory");
  });
  const int rt32 = rb * 4 + rsb;

  const int xrow = 32 * rsb + l31, xsw = (xrow >> 1) & 7;
  lds_cptr xb[4];
#pragma unroll
  for (int j = 0; j < 4; ++j) xb[j] = L + xrow * 128 + 16 * (((j << 1) | hh) ^ xsw);
  const int rowo = (8 * hh + q4) * 128 + 8 * p4;
  const lds_cptr wt0 = L + 16384 + rowo + 32 * ((2 * cg + blk) ^ q4);
  const lds_cptr wt1 = VB ? wt0 + 8192 : L + 24576 + 8192 * blk + rowo + 32 * ((2 * cg) ^ q4 ^ blk);
  const lds_cptr wt2 = VB ? wt0 + 16384 : L + 24576 + 8192 * blk + rowo + 32 * ((2 * cg + 1) ^ q4 ^ blk);
  const int rowb = hh * 2048 + q4 * 128 + 8 * p4;
  const lds_cptr wb0 = VB ? L + rowb + 32 * ((2 * cg + blk) ^ q4) : L + rowb + blk * 1024 + 32 * ((2 * cg) ^ q4 ^ blk);
  const lds_cptr wb1 = L + rowb + blk * 1024 + 32 * ((2 * cg + 1) ^ q4 ^ blk);

  bf16x8 xf[28];
  f32x16 a0, a1, a2, b0, b1, zero16;
#pragma unroll
  for (int i = 0; i < 16; ++i) zero16[i] = 0.f;
  bf16x8 wf[6];

  auto rdA = [&](auto I) {
    constexpr int i = decltype(I)::v, s = i >> 2, j = i & 3, sb = pos1(s), w = (i & 1) * 3;
    xf[i] = ldsread16(xb[j] + sb);
    wf[w + 0] = cat8(trread(wt0 + sb + j * 2048), trread(wt0 + sb + j * 2048 + 512));
    wf[w + 1] = cat8(trread(wt1 + sb + j * 2048), trread(wt1 + sb + j * 2048 + 512));
    wf[w + 2] = cat8(trread(wt2 + sb + j * 2048), trread(wt2 + sb + j * 2048 + 512));
  };
  auto rdB = [&](auto I) {
    constexpr int i = decltype(I)::v, kk = i % 28;
    const lds_cptr b = i < 28 ? wb0 : wb1;
    wf[i % 4 < 3 ? i % 4 : 3] = cat8(trread(b + kk * 4096), trread(b + kk * 4096 + 512));
  };

  waitv_bar<15>();
  PIN(genv); PIN(gx); PIN(gwv);
  const unsigned epoch = __builtin_amdgcn_readfirstlane(genv) * 1664525u + 1013904223u;
  float gs = 0.f;
  STAMP_AT(A.ws, 1);
  rdA(IC<0>{});
  sfor<0, 28>([&](auto I) {
    constexpr int i = decltype(I)::v, s = i >> 2;
    if constexpr ((i & 3) == 3) waitv_bar<(s < 6 ? 10 : 9)>();
    if constexpr (i < 27) rdA(IC<i + 1>{});
    else { rdB(IC<0>{}); rdB(IC<1>{}); rdB(IC<2>{}); }
    SBAR();
    constexpr int w = (i & 1) * 3;
    if constexpr (VB) a0 = MFMA32(xf[i], wf[w], i == 0 ? zero16 : a0); else a0 = MFMA32(wf[w], xf[i], i == 0 ? zero16 : a0);
    SBAR();
    if constexpr ((i & 3) == 3) piece(IC<s + 4>{}, IC<0>{});
    else if constexpr (s >= 1) piece(IC<s + 3>{}, IC<(i & 3) + 2>{});
    SBAR();
    if constexpr (VB) a1 = MFMA32(xf[i], wf[w + 1], i == 0 ? zero16 : a1); else a1 = MFMA32(wf[w + 1], xf[i], i == 0 ? zero16 : a1);
    SBAR();
    if constexpr ((i & 3) == 3) piece(IC<s + 4>{}, IC<1>{});
    SBAR();
    if constexpr (VB) a2 = MFMA32(xf[i], wf[w + 2], i == 0 ? zero16 : a2); else a2 = MFMA32(wf[w + 2], xf[i], i == 0 ? zero16 : a2);
    if constexpr (i >= 1 && i < 5) {
      gs += rbf(__uint_as_float(gx[i - 1] << 16) * __uint_as_float(gwv[i - 1] << 16));
      gs += rbf(__uint_as_float(gx[i - 1] & 0xffff0000u) * __uint_as_float(gwv[i - 1] & 0xffff0000u));
      if constexpr (i == 4) gs = lane < 56 ? gs : 0.f;
      PIN(gs);
    }
    if constexpr (i >= 5 && i < 11) { gs += __shfl_xor(gs, 1 << (i - 5)); PIN(gs); }
    if constexpr (i == 12) { if (lane == 0) st4(rs, WS_G + 4 * grow, __float_as_uint(rbf(1.f / (1.f + __expf(-rbf(gs)))))); }
    SBAR();
  });
  STAMP_AT(A.ws, 2);

  if constexpr (VB) {
    auto vstore = [&](const f32x16& a, int line) {
      const int n = 64 * (line - 14) + 32 * cg + l31, head = n / 28, dh = n - 28 * head;
      const unsigned o = WS_KV + ((head * 64 + rt32) * 4 + 2) * 1024 + (dh + 32 * hh) * 16;
      float v[16];
#pragma unroll
      for (int i = 0; i < 16; ++i) v[i] = a[i];
      st16(rs, o, pack8(v)); st16(rs, o + 1024, pack8(v + 8));
    };
    const int l0 = cgp == 14 ? 14 : 18;
    {
      const int head = 8 * (cgp - 14) + 4 * cg + (lane >> 4);
      const unsigned o = WS_KV + ((head * 64 + rt32) * 4 + 2 + ((lane >> 3) & 1)) * 1024 + (28 + (lane & 3) + 32 * ((lane >> 2) & 1)) * 16;
      const unsigned v = (lane & 3) == 0 ? 0x3f803f80u : 0u;
      st16(rs, o, u32x4{v, v, v, v});
    }
    sfor<0, 28>([&](auto I) {
      constexpr int i = decltype(I)::v;
      if constexpr (i == 7) waitv_bar<4 + 1 + 4>();
      if constexpr (i == 17) waitv_bar<7>();
      if constexpr (i + 3 < 28) rdB(IC<i + 3>{});
      SBAR();
      b0 = MFMA32(xf[i], wf[i % 4], i == 0 ? zero16 : b0);
      if constexpr (i == 2) vstore(a0, l0);
      if constexpr (i == 5) vstore(a1, l0 + 1);
      if constexpr (i == 9) vstore(a2, l0 + 2);
      SBAR();
    });
    if (cgp == 14) vstore(b0, 17);
    waitv_bar<0>();
    if (tid == 0) st4(rs, F_QKV + 4 * blockIdx.x, epoch);
  } else {
    float hv[8];
    u32x4 qk[2];
    const bool isq = cgp < 7;
    const float qs = isq ? QSCALE : 1.f;
    const unsigned hbase = WS_H + (rt32 * 112 + 8 * cgp + 2 * cg) * 1024 + lane * 16;
    sfor<0, 56>([&](auto I) {
      constexpr int i = decltype(I)::v, kk = i % 28;
      if constexpr (i == 7) waitv_bar<6>();
      if constexpr (i == 17) waitv_bar<3>();
      if constexpr (i == 30) { waitv_bar<2>(); if (tid == 0) st4(rs, F_QKV + 4 * blockIdx.x, epoch); }
      if constexpr (i + 3 < 56) rdB(IC<i + 3>{});
      SBAR();
      if constexpr (i < 28) b0 = MFMA32(wf[i % 4], xf[kk], kk == 0 ? zero16 : b0);
      else b1 = MFMA32(wf[i % 4], xf[kk], kk == 0 ? zero16 : b1);
      if constexpr (i == 1 || i == 2) {
        constexpr int t = i - 1;
        const unsigned w0 = pk2(a0[8 * t] * qs, a0[8 * t + 1] * qs), w1 = pk2(a0[8 * t + 2] * qs, a0[8 * t + 3] * qs);
        const unsigned w2 = pk2(a0[8 * t + 4] * qs, a0[8 * t + 5] * qs), w3 = pk2(a0[8 * t + 6] * qs, a0[8 * t + 7] * qs);
        auto r0 = __builtin_amdgcn_permlane32_swap(w0, w2, false, false);
        auto r1 = __builtin_amdgcn_permlane32_swap(w1, w3, false, false);
        qk[t] = u32x4{r0[0], r1[0], r0[1], r1[1]};
        PIN(qk[t]);
      }
      if constexpr (i == 3 || i == 4) {
        constexpr int t = i - 3;
        const int P = 8 * (isq ? cgp : cgp - 7) + 4 * cg + 2 * t + hh;
        const int hp = P / 7, r = P - 7 * hp;
        const int head = 2 * hp + (r > 3 ? 1 : 0), piece = r > 3 ? r - 4 : r;
        const unsigned hs = isq ? 2048u : 4096u, b = (isq ? WS_Q : WS_KV) + rt32 * hs + l31 * 16;
        const unsigned pad = isq ? 0u : 0x3f80u;
        u32x4 v = qk[t];
        if (r == 3) { v[2] = pad; v[3] = 0u; }
        st16(rs, b + head * 64 * hs + (piece >> 1) * 1024 + (piece & 1) * 512, v);
        if (r == 3) st16(rs, b + (head + 1) * 64 * hs + 1536, u32x4{qk[t][2], qk[t][3], pad, 0u});
      }
      if constexpr (i >= 6 && i < 14) { hv[i - 6] = sigm_mul(a1[i - 6], a1[i + 2]); PIN(hv[i - 6]); }
      if constexpr (i == 14) st16(rs, hbase, pack8(hv));
      if constexpr (i >= 15 && i < 23) { hv[i - 15] = sigm_mul(a2[i - 15], a2[i - 7]); PIN(hv[i - 15]); }
      if constexpr (i == 23) st16(rs, hbase + 1024, pack8(hv));
      if constexpr (i >= 31 && i < 39) { hv[i - 31] = sigm_mul(b0[i - 31], b0[i - 23]); PIN(hv[i - 31]); }
      if constexpr (i == 39) st16(rs, hbase + 4096, pack8(hv));
      SBAR();
    });
#pragma unroll
    for (int i = 0; i < 8; ++i) hv[i] = sigm_mul(b1[i], b1[i + 8]);
    st16(rs, hbase + 5120, pack8(hv));
  }
  STAMP_AT(A.ws, 3);
  return epoch;
}

constexpr int RING0 = 114688, KVST = 16384;

DI float xhalf_max(float v) {
  auto r = __builtin_amdgcn_permlane32_swap(__float_as_uint(v), __float_as_uint(v), false, false);
  return fmaxf(__uint_as_float(r[0]), __uint_as_float(r[1]));
}
DI float max16(const f32x16& s) {
  float a = fmaxf(fmaxf(s[0], s[1]), s[2]), b = fmaxf(fmaxf(s[3], s[4]), s[5]);
  a = fmaxf(fmaxf(a, s[6]), s[7]); b = fmaxf(fmaxf(b, s[8]), s[9]);
  a = fmaxf(fmaxf(a, s[10]), s[11]); b = fmaxf(fmaxf(b, s[12]), s[13]);
  a = fmaxf(fmaxf(a, s[14]), s[15]);
  return fmaxf(a, b);
}

DI void phase23(const Args& A, rsrc_t rs, char* lds, int rb1, int cgp, unsigned epoch) {
  const int bid = blockIdx.x, xcd = bid & 7, idx = bid >> 3;
  const int hd = 2 * xcd + (idx & 1), rb = idx >> 1;
  const int i3 = idx < 28 ? idx : idx - 28, rt64 = 4 * xcd + i3 / 7, c7 = i3 % 7;
  const int tid = threadIdx.x, lane = tid & 63, wid = __builtin_amdgcn_readfirstlane(tid >> 6);
  const int hh = lane >> 5, qs = wid & 3, kh = wid >> 2;
  const unsigned lds0 = (unsigned)(uintptr_t)lds;
  const lds_cptr L = (lds_cptr)(LAS char*)(uintptr_t)lds0;
  const unsigned kvo = WS_KV + hd * (64 * 4096) + kh * (32 * 4096);
  const char* kvh = A.ws + kvo;
  const int rot = 2 * rb;
  const unsigned l16 = lane * 16;
  auto stage = [&](int s, int slot, int p) {
    const int lt = 2 * s + (qs >> 1) + ((qs & 1) ? 0 : 2);
    glds16_dev(kvh + (size_t)((lt + rot) & 31) * 4096 + (qs & 1) * 2048 + p * 1024, l16, lds0 + RING0 + slot * KVST + kh * 8192 + qs * 2048 + p * 1024);
  };
  const int r3 = wid & 1, kq = wid >> 1;
  const int r8 = lane >> 3, c8 = lane & 7;
  const unsigned ow = r8 * XROW + 32 * ((c8 >> 1) ^ (r8 & 3)) + 16 * (c8 & 1);
  const char* wds = A.wd + (size_t)(448 * kq + 16 * r3) * XROW + 128 * c7;
  auto stage_wd = [&](int c, int slot, int p) {
    glds16(wds + (size_t)(32 * c + 8 * p) * XROW, ow, lds0 + slot * 16384 + wid * 2048 + p * 1024);
  };

  wait_flags256(rs, F_QKV, epoch, lane);
  waitv_bar<0>();
  if (tid == 0) st4(rs, F_H + 128 * rb1 + 4 * cgp, epoch);
  STAMP_AT(A.ws, 4);
  stage(0, 0, 0); stage(0, 0, 1); stage(1, 1, 0); stage(1, 1, 1);
  const unsigned qo = WS_Q + ((hd * 64 + rb * 4 + qs) * 2) * 1024 + lane * 16;
  const bf16x8 q0 = ldfrag(rs, qo);
  bf16x8 q1 = ldfrag(rs, qo + 1024);
  const unsigned k0o = kvo + (rot & 31) * 4096 + lane * 16, k1o = kvo + ((rot + 1) & 31) * 4096 + lane * 16;
  const bf16x8 ka0 = ldfrag(rs, k0o), ka1 = ldfrag(rs, k0o + 1024);
  bf16x8 kf0 = ldfrag(rs, k1o), kf1 = ldfrag(rs, k1o + 1024);
  f32x16 zero16;
#pragma unroll
  for (int i = 0; i < 16; ++i) zero16[i] = 0.f;
  f32x16 sA = MFMA32(ka0, q0, zero16);
  float mref;
  {
    f32x16 s = MFMA32(ka1, q1, sA);
    mref = rbf(xhalf_max(max16(s)));
  }
  bf16x8 q1m = q1;
  if (hh) q1m[4] = (short)((__float_as_uint(-mref)) >> 16);
  sA = MFMA32(ka1, q1m, sA);
  PIN(kf0); PIN(kf1);
  STAMP_AT(A.ws, 5);
  f32x16 o = zero16, sB;
  bf16x8 vf0, vf1;
  u32x4 p0 = {0, 0, 0, 0}, p1 = {0, 0, 0, 0};
  float e0 = 0.f, e1 = 0.f, e2 = 0.f, e3 = 0.f;
#pragma unroll
  for (int i = 0; i < 8; ++i) vf1[i] = 0;
  const lds_cptr rb0 = L + RING0 + kh * 8192 + lane * 16;
  bf16x8 hf[28];
  const unsigned ho = WS_H + ((rt64 * 2 + r3) * 112 + 28 * kq) * 1024 + lane * 16;
#define G(MF, RDS, W0, W1, C, i) do { MF; SBAR(); RDS; SBAR(); \
    float n0 = EX2(C[i]); PIN(n0); SBAR(); W0 = pk2(e0, e1); PIN(W0); SBAR(); float n1 = EX2(C[i + 1]); PIN(n1); SBAR(); W1 = pk2(e2, e3); PIN(W1); SBAR(); \
    float n2 = EX2(C[i + 2]); PIN(n2); float n3 = EX2(C[i + 3]); PIN(n3); e0 = n0; e1 = n1; e2 = n2; e3 = n3; SBAR(); } while (0)
#define STEP(C, N, b, PA, PB, X0, X1, X2) do { \
    __builtin_amdgcn_s_setprio(PA); \
    G(N = MFMA32(kf0, q0, zero16), X0, p1[2], p1[3], C, 0); \
    G(o = MFMA32(vf1, __builtin_bit_cast(bf16x8, p1), o), vf0 = ldsread16((b) + 2048); X1, p0[0], p0[1], C, 4); \
    __builtin_amdgcn_s_setprio(PB); \
    G(N = MFMA32(kf1, q1m, N), vf1 = ldsread16((b) + 3072); kf0 = ldsread16((b)); kf1 = ldsread16((b) + 1024), p0[2], p0[3], C, 8); \
    G(o = MFMA32(vf0, __builtin_bit_cast(bf16x8, p0), o), X2, p1[0], p1[1], C, 12); \
  } while (0)
  sfor<0, 16>([&](auto S) {
    constexpr int s = decltype(S)::v;
    constexpr auto E = [](int t) { return t < 0 ? 0 : t < 7 ? 2 : (t >= 8 && t < 15) ? 4 : 0; };
    constexpr int N = (s + 1 < 16 ? 2 : 0) + E(s - 2) + E(s - 1);
    waitv_bar<N>();
    if constexpr (s == 8) wait_flags(rs, F_H + 128 * (rt64 >> 1), 16, epoch, lane);
    auto vm = [&](auto J) {
      constexpr int j = decltype(J)::v;
      if constexpr (j < 2) { if constexpr (s + 2 < 16) stage(s + 2, (s + 2) % 3, j); }
      else if constexpr (s < 7) { if constexpr (j < 4) stage_wd(s, s, j - 2); }
      else if constexpr (s >= 8 && s < 15) hf[4 * (s - 8) + j - 2] = ldfrag(rs, ho + (4 * (s - 8) + j - 2) * 1024);
    };
    SBAR();
    const lds_cptr b = rb0 + (s % 3) * KVST;
    STEP(sA, sB, b, 3, 2, vm(IC<0>{}), vm(IC<1>{}), vm(IC<2>{}));
    STEP(sB, sA, b + 4096, 1, 0, vm(IC<3>{}), vm(IC<4>{}), vm(IC<5>{}));
  });
  p1[2] = pk2(e0, e1); p1[3] = pk2(e2, e3);
  o = MFMA32(vf1, __builtin_bit_cast(bf16x8, p1), o);
#undef STEP
#undef G
  STAMP_AT(A.ws, 6);
  {
    bool bad = false;
#pragma unroll
    for (int i = 0; i < 13; ++i) bad |= !(fabsf(o[i]) <= 1.0e30f);
#pragma unroll
    for (int i = 13; i < 16; ++i) bad |= !hh && !(fabsf(o[i]) <= 1.0e30f);
    if (__any(bad)) {
      float m = -INFINITY;
      o = zero16;
#pragma unroll 1
      for (int t = 0; t < 32; ++t) {
        const unsigned p = kvo + t * 4096 + lane * 16;
        f32x16 s = MFMA32(ldfrag(rs, p), q0, zero16);
        s = MFMA32(ldfrag(rs, p + 1024), q1, s);
        const float mn = fmaxf(m, xhalf_max(max16(s)));
        const float al = EX2(m - mn);
        m = mn;
        float e[16];
#pragma unroll
        for (int i = 0; i < 16; ++i) { o[i] *= al; e[i] = EX2(s[i] - mn); }
        o = MFMA32(ldfrag(rs, p + 2048), __builtin_bit_cast(bf16x8, pack8(e)), o);
        o = MFMA32(ldfrag(rs, p + 3072), __builtin_bit_cast(bf16x8, pack8(e + 8)), o);
      }
      mref = m;
    }
  }
  __syncthreads();
  f32x4* xb = (f32x4*)(lds + RING0) + qs * (5 * 64);
  if (kh == 1) {
#pragma unroll
    for (int i = 0; i < 4; ++i) xb[i * 64 + lane] = f32x4{o[4 * i], o[4 * i + 1], o[4 * i + 2], o[4 * i + 3]};
    xb[4 * 64 + lane] = f32x4{mref, 0.f, 0.f, 0.f};
  }
  __syncthreads();
  if (kh == 0) {
    const float m1 = xb[4 * 64 + lane][0];
    const float M = fmaxf(mref, m1);
    const float a0 = EX2(mref - M), a1 = EX2(m1 - M);
#pragma unroll
    for (int i = 0; i < 4; ++i) {
      const f32x4 t = xb[i * 64 + lane];
#pragma unroll
      for (int j = 0; j < 4; ++j) o[4 * i + j] = o[4 * i + j] * a0 + t[j] * a1;
    }
    auto r = __builtin_amdgcn_permlane32_swap(__float_as_uint(o[12]), __float_as_uint(o[12]), false, false);
    const float inv = 1.f / __uint_as_float(r[1]);
    float v[16];
#pragma unroll
    for (int i = 0; i < 16; ++i) v[i] = o[i] * inv;
    if (hh) { v[12] = 0.f; v[13] = 0.f; v[14] = 0.f; v[15] = 0.f; }
    const unsigned dst = WS_O + (((rb * 4 + qs) * 16 + hd) * 2) * 1024 + lane * 16;
    st16(rs, dst, pack8(v)); st16(rs, dst + 1024, pack8(v + 8));
  }
  waitv_bar<0>();
  if (tid == 0) st4(rs, F_O + 128 * rb + 4 * hd, epoch);
  STAMP_AT(A.ws, 7);
  if (idx >= 28) {
    if (bid == 255 && wid == 0) {
      if (lane == 0) st4(rs, F_GEN, epoch);
    }
    return;
  }

  const int q4 = (lane & 15) >> 2, p4 = lane & 3, blk = (lane >> 4) & 1;
  const int wro = (4 * hh + q4) * 128 + 8 * p4;
  const lds_cptr wd0 = L + kq * 4096 + wro + 32 * ((0 + blk) ^ q4), wd1 = L + kq * 4096 + wro + 32 * ((2 + blk) ^ q4);
  f32x16 bc0 = zero16, bc1 = zero16;
  bf16x8 wf[4];
  auto rdW = [&](auto I) {
    constexpr int i = decltype(I)::v, so = ((i >> 1) % 10) * 16384 + (i & 1) * 2048;
    wf[(i & 1) * 2] = cat8(trread(wd0 + so), trread(wd0 + so + 1024));
    wf[(i & 1) * 2 + 1] = cat8(trread(wd1 + so), trread(wd1 + so + 1024));
  };
  const char* wos = A.wo + (size_t)(56 * wid) * XROW + 128 * c7;
  rdW(IC<0>{});
  sfor<0, 28>([&](auto I) {
    constexpr int i = decltype(I)::v;
    if constexpr (i == 7) waitv_bar<6>();
    if constexpr (i == 13) waitv_bar<8>();
    if constexpr (i == 15) waitv_bar<8>();
    if constexpr (i == 19) waitv_bar<7>();
    if constexpr (i < 27) rdW(IC<i + 1>{});
    SBAR();
    auto vm = [&](auto U) {
      constexpr int u = decltype(U)::v;
      if constexpr (i < 3) stage_wd(7 + i, 7 + i, u);
      if constexpr (i >= 7 && i < 11) stage_wd(3 + i, i - 7, u);
      if constexpr (i >= 15 && i < 19 && 2 * (i - 15) + u < 7) glds16(wos + (size_t)(8 * (2 * (i - 15) + u)) * XROW, ow, lds0 + 65536 + wid * 7168 + (2 * (i - 15) + u) * 1024);
    };
    bc0 = MFMA32(wf[(i & 1) * 2], hf[i], bc0);
    SBAR(); vm(IC<0>{}); SBAR();
    bc1 = MFMA32(wf[(i & 1) * 2 + 1], hf[i], bc1);
    SBAR(); vm(IC<1>{}); SBAR();
  });
  STAMP_AT(A.ws, 8);
  wait_flags(rs, F_O + 128 * (rt64 >> 1) + 16 * kq, 4, epoch, lane);
  STAMP_AT(A.ws, 9);
  bf16x8 of[8];
  const unsigned oo = WS_O + (((rt64 * 2 + r3) * 16 + 4 * kq) * 2) * 1024 + lane * 16;
#pragma unroll
  for (int i = 0; i < 8; ++i) of[i] = ldfrag(rs, oo + i * 1024);
  const float g = __uint_as_float(ld4(rs, WS_G + 4 * (rt64 * 64 + r3 * 32 + (lane & 31))));
  waitv_bar<0>();
  STAMP_AT(A.ws, 10);
  f32x16 ac0 = zero16, ac1 = zero16;
  {
    const lds_cptr wo0 = L + 65536 + kq * (112 * 128) + wro + 32 * ((0 + blk) ^ q4), wo1 = L + 65536 + kq * (112 * 128) + wro + 32 * ((2 + blk) ^ q4);
    const int hi1 = hh ? 2048 : 3072;
#pragma unroll
    for (int h = 0; h < 4; ++h) {
      const int b = h * 28 * 128;
      ac0 = MFMA32(cat8(trread(wo0 + b), trread(wo0 + b + 1024)), of[2 * h], ac0);
      ac1 = MFMA32(cat8(trread(wo1 + b), trread(wo1 + b + 1024)), of[2 * h], ac1);
      ac0 = MFMA32(cat8(trread(wo0 + b + 2048), trread(wo0 + b + hi1)), of[2 * h + 1], ac0);
      ac1 = MFMA32(cat8(trread(wo1 + b + 2048), trread(wo1 + b + hi1)), of[2 * h + 1], ac1);
    }
  }
  const float gm = rbf(1.f - g);
  __syncthreads();
  {
    char* rp = lds + kq * (64 * 272) + (r3 * 32 + (lane & 31)) * 272 + 16 * hh;
#pragma unroll
    for (int i = 0; i < 4; ++i) {
      *(f32x4*)(rp + 32 * i) = f32x4{g * ac0[4 * i] + gm * bc0[4 * i], g * ac0[4 * i + 1] + gm * bc0[4 * i + 1], g * ac0[4 * i + 2] + gm * bc0[4 * i + 2], g * ac0[4 * i + 3] + gm * bc0[4 * i + 3]};
      *(f32x4*)(rp + 128 + 32 * i) = f32x4{g * ac1[4 * i] + gm * bc1[4 * i], g * ac1[4 * i + 1] + gm * bc1[4 * i + 1], g * ac1[4 * i + 2] + gm * bc1[4 * i + 2], g * ac1[4 * i + 3] + gm * bc1[4 * i + 3]};
    }
  }
  __syncthreads();
  {
    const int row = tid >> 3, cc = tid & 7;
    const char* rp = lds + row * 272 + cc * 32;
    f32x4 s0 = *(const f32x4*)rp, s1 = *(const f32x4*)(rp + 16);
#pragma unroll
    for (int k = 1; k < 4; ++k) { s0 += *(const f32x4*)(rp + k * (64 * 272)); s1 += *(const f32x4*)(rp + k * (64 * 272) + 16); }
    u32x4 w = {pk2(s0[0], s0[1]), pk2(s0[2], s0[3]), pk2(s1[0], s1[1]), pk2(s1[2], s1[3])};
    *(u32x4*)(A.out + (size_t)(rt64 * 64 + row) * XROW + c7 * 128 + cc * 16) = w;
  }
  STAMP_AT(A.ws, 11);
}

__global__ __launch_bounds__(512, 2) void fused_kernel(Args A) {
  extern __shared__ __attribute__((aligned(16))) char lds[];
  const int bid = blockIdx.x, xcd = bid & 7, idx = bid >> 3;
  const int rb1 = 8 * (xcd & 1) + (idx & 7), cgp = 4 * (xcd >> 1) + (idx >> 3);
  STAMP_AT(A.ws, 0);
  const rsrc_t rs = __builtin_amdgcn_make_buffer_rsrc((void*)A.ws, 0, WS_END, 0x00020000);
  unsigned epoch;
  if (cgp < 14) epoch = phase1<false>(A, rs, lds, rb1, cgp);
  else epoch = phase1<true>(A, rs, lds, rb1, cgp);
  phase23(A, rs, lds, rb1, cgp, epoch);
}

extern "C" void kernel_launch(void* const* d_in, const int* in_sizes, int n_in, void* d_out, int out_size, void* d_ws, size_t ws_size,
                              hipStream_t stream) {
  Args A;
  A.x = (const char*)d_in[0]; A.wqkv = (const char*)d_in[1]; A.wo = (const char*)d_in[2];
  A.wg = (const char*)d_in[4]; A.wu = (const char*)d_in[5]; A.wd = (const char*)d_in[6]; A.gw = (const char*)d_in[7];
  A.ws = (char*)d_ws; A.out = (char*)d_out;
  (void)hipFuncSetAttribute((const void*)fused_kernel, hipFuncAttributeMaxDynamicSharedMemorySize, LDS_BYTES);
  hipLaunchKernelGGL(fused_kernel, dim3(256), dim3(512), LDS_BYTES, stream, A);
}
```

```cpp
#include <hip/hip_runtime.h>
#include <hip/hip_bf16.h>
#include <stdint.h>

typedef __attribute__((ext_vector_type(8))) short bf16x8;
typedef __attribute__((ext_vector_type(4))) short s16x4;
typedef __attribute__((ext_vector_type(16))) float f32x16;
typedef __attribute__((ext_vector_type(4))) float f32x4;
typedef __attribute__((ext_vector_type(4))) unsigned u32x4;
typedef __attribute__((ext_vector_type(2))) unsigned u32x2;
typedef __attribute__((ext_vector_type(2))) float f32x2;
typedef __attribute__((ext_vector_type(2))) __bf16 bf16x2_t;
#define LAS __attribute__((address_space(3)))
#define GAS __attribute__((address_space(1)))
typedef LAS const char* lds_cptr;
typedef LAS char* lds_ptr;
typedef __amdgpu_buffer_rsrc_t rsrc_t;

#define DI __device__ __forceinline__
#define MFMA32(a, b, c) __builtin_amdgcn_mfma_f32_32x32x16_bf16((a), (b), (c), 0, 0, 0)
#define SBAR() __builtin_amdgcn_sched_barrier(0)
#define PIN(x) asm volatile("" : "+v"(x))
#define EX2(x) __builtin_amdgcn_exp2f(x)

#define STAMP_F(ws, slot) do {} while (0)
#define STAMP_ARG
#define STAMP_PASS
#define STAMP_AT(ws, slot) do {} while (0)
#define STAMP_FLUSH(ws) do {} while (0)

constexpr int D_ = 448, F_ = 1792;
constexpr int XROW = D_ * 2, QROW = 3 * D_ * 2, FROW = F_ * 2;
constexpr float QSCALE = 0.18898223650461362f * 1.4426950408889634f;
constexpr float LOG2E = 1.4426950408889634f;

constexpr unsigned WS_Q = 0;
constexpr unsigned WS_KV = WS_Q + (2u << 20);
constexpr unsigned WS_H = WS_KV + (4u << 20);
constexpr unsigned WS_O = WS_H + (7u << 20);
constexpr unsigned WS_G = WS_O + (2u << 20);
constexpr unsigned WS_F = WS_G + 8192;
constexpr unsigned F_GEN = WS_F, F_H = WS_F + 4096, F_O = WS_F + 8192, F_QKV = WS_F + 16384;
constexpr unsigned WS_END = WS_F + 32768;

template <int I> struct IC { static constexpr int v = I; };
template <int B, int E, class Fn> DI void sfor(Fn&& f) {
  if constexpr (B < E) { f(IC<B>{}); sfor<B + 1, E>(f); }
}

DI unsigned pk2(float lo, float hi) {
  f32x2 v = {lo, hi};
  bf16x2_t r = __builtin_convertvector(v, bf16x2_t);
  return __builtin_bit_cast(unsigned, r);
}
DI float rbf(float x) { return __uint_as_float(pk2(x, 0.f) << 16); }
DI u32x4 pack8(const float* v) { return u32x4{pk2(v[0], v[1]), pk2(v[2], v[3]), pk2(v[4], v[5]), pk2(v[6], v[7])}; }

DI void glds16(const char* base, unsigned voff, unsigned lds_base) {
  asm volatile("s_mov_b32 m0, %2\n\ts_nop 0\n\tglobal_load_lds_dwordx4 %0, %1" ::"v"(voff), "s"(base), "s"(lds_base) : "memory");
}
DI void glds16_dev(const char* base, unsigned voff, unsigned lds_base) {
  asm volatile("s_mov_b32 m0, %2\n\ts_nop 0\n\tglobal_load_lds_dwordx4 %0, %1 sc1" ::"v"(voff), "s"(base), "s"(lds_base) : "memory");
}
typedef short v4i16_t __attribute__((ext_vector_type(4)));
DI s16x4 trread(lds_cptr p) {
  return __builtin_bit_cast(s16x4, __builtin_amdgcn_ds_read_tr16_b64_v4i16((LAS v4i16_t*)p));
}
DI bf16x8 ldsread16(lds_cptr p) { return *(const LAS bf16x8*)p; }
DI bf16x8 cat8(s16x4 a, s16x4 b) { return __builtin_shufflevector(a, b, 0, 1, 2, 3, 4, 5, 6, 7); }
template <int N> DI void waitv_bar() {
  SBAR();
  asm volatile("s_waitcnt vmcnt(%0) lgkmcnt(0)\n\ts_barrier" ::"i"(N) : "memory");
  SBAR();
}
template <int N> DI void waitv_bar_nl() {
  SBAR();
  asm volatile("s_waitcnt vmcnt(%0)\n\ts_barrier" ::"i"(N) : "memory");
  SBAR();
}
DI void st16(rsrc_t r, unsigned off, u32x4 v) { __builtin_amdgcn_raw_buffer_store_b128(v, r, off, 0, 16); }
DI void st4(rsrc_t r, unsigned off, unsigned v) { __builtin_amdgcn_raw_buffer_store_b32(v, r, off, 0, 16); }
DI u32x4 ld16(rsrc_t r, unsigned off) { return __builtin_amdgcn_raw_buffer_load_b128(r, off, 0, 16); }
DI unsigned ld4(rsrc_t r, unsigned off) { return __builtin_amdgcn_raw_buffer_load_b32(r, off, 0, 16); }
DI bf16x8 ldfrag(rsrc_t r, unsigned off) { return __builtin_bit_cast(bf16x8, ld16(r, off)); }

struct Args {
  const char *x, *wqkv, *wo, *wg, *wu, *wd, *gw;
  char* ws;
  char* out;
};

constexpr int LDS_BYTES = 163840;
constexpr int SLOT = 40960;
DI constexpr int pos1(int s) { return (s % 3) * SLOT; }

constexpr int QKV_STEP = 34, O_STEP = 44;
constexpr int SPIN_CAP = 100000;
DI void wait_flags(rsrc_t rs, unsigned off, int n, unsigned epoch, int lane) {
  const unsigned o = off + 4 * (lane < n ? lane : 0);
  for (int spin = 0; spin < SPIN_CAP; ++spin) {
    const unsigned v = ld4(rs, o);
    if (__all(v == epoch)) break;
    __builtin_amdgcn_s_sleep(4);
  }
  asm volatile("" ::: "memory");
}
DI void wait_flags256(rsrc_t rs, unsigned off, unsigned epoch, int lane) {
  for (int spin = 0; spin < SPIN_CAP; ++spin) {
    const u32x4 v = ld16(rs, off + 16 * lane);
    if (__all(v[0] == epoch && v[1] == epoch && v[2] == epoch && v[3] == epoch)) break;
    __builtin_amdgcn_s_sleep(8);
  }
  asm volatile("" ::: "memory");
}

DI unsigned look16(const char* p, int wid, int lane) {
  unsigned v = 0;
  asm volatile("s_cmp_eq_u32 %3, 0\n\ts_cselect_b64 exec, -1, 0\n\tglobal_load_dword %0, %1, %2 sc1\n\ts_mov_b64 exec, -1"
               : "+v"(v) : "v"(4 * (lane & 15)), "s"(p), "s"(wid) : "memory", "scc");
  return v;
}
DI float sigm_mul(float g, float u) { return u * __builtin_amdgcn_rcpf(1.f + EX2(-LOG2E * g)); }

constexpr int RING0 = 114688, KVST = 16384;
DI void kv_stage(const char* ws, unsigned lds0, int wid, unsigned l16, int s, int slot, int p) {
  const int bid = blockIdx.x, idx = bid >> 3, hd = 2 * (bid & 7) + (idx & 1), rot = 2 * (idx >> 1);
  const int qs = wid & 3, kh = wid >> 2;
  const int lt = 2 * s + (qs >> 1) + ((qs & 1) ? 0 : 2);
  glds16_dev(ws + WS_KV + hd * (64 * 4096) + kh * (32 * 4096) + ((lt + rot) & 31) * 4096 + (qs & 1) * 2048 + p * 1024, l16,
             lds0 + RING0 + slot * KVST + kh * 8192 + qs * 2048 + p * 1024);
}
DI void p2_first(const char* ws, unsigned lds0, int wid, unsigned l16, int j) {
  if (j >= 2) { kv_stage(ws, lds0, wid, l16, (j - 2) >> 1, (j - 2) >> 1, j & 1); return; }
  const int bid = blockIdx.x, idx = bid >> 3, hd = 2 * (bid & 7) + (idx & 1), rb = idx >> 1;
  const unsigned o = wid < 4 ? WS_KV + hd * (64 * 4096) + (wid >> 1) * (32 * 4096) + (((wid & 1) + 2 * rb) & 31) * 4096
                             : WS_Q + (hd * 64 + rb * 4 + (wid - 4)) * 2048;
  glds16_dev(ws + o + j * 1024, l16, lds0 + RING0 + 2 * KVST + wid * 2048 + j * 1024);
}
DI void set_qkv_flag(rsrc_t rs, unsigned epoch) {
  if (threadIdx.x < 8) st4(rs, F_QKV + 1024 * threadIdx.x + 4 * blockIdx.x, epoch);
}

DI unsigned touch(const Args& A, int wid, int lane, int r) {
  const int w = blockIdx.x * 8 + wid;
  const char* p;
  if (r == 0) p = w < 1792 ? A.x + (size_t)w * 1024 : A.wqkv + (size_t)(w - 1792) * 1024;
  else if (r == 1) p = A.wqkv + (size_t)(256 + (w < 920 ? w : w < 1840 ? w - 920 : w - 1840)) * 1024;
  else if (r < 4) {
    int g = w + 1792 * (r - 2); if (g >= 3136) g -= 1024;
    p = ((g & 1) ? A.wu : A.wg) + (size_t)(g >> 1) * 1024;
  }
  else p = w < 1568 ? A.wd + (size_t)w * 1024 : A.wo + (size_t)(w < 1960 ? w - 1568 : w - 1960) * 1024;
  unsigned v;
  asm volatile("global_load_dword %0, %1, %2" : "=v"(v) : "v"((lane & 7) * 128), "s"(p) : "memory");
  return v;
}
DI unsigned load_gen(const Args& A) {
  unsigned v;
  asm volatile("global_load_dword %0, %1, %2 sc1" : "=v"(v) : "v"(0), "s"(A.ws + F_GEN) : "memory");
  return v;
}
DI unsigned epoch_of(unsigned genv) { return __builtin_amdgcn_readfirstlane(genv) * 1664525u + 1013904223u; }

DI constexpr int posA(int s) { return s == 0 || s == 6 ? 139264 : 16384 + (s - 1) * 24576; }
DI unsigned phase1_gu(const Args& A, rsrc_t rs, char* lds, int rb, int cgp, unsigned& tvw STAMP_ARG) {
  const int tid = threadIdx.x, lane = tid & 63, wid = __builtin_amdgcn_readfirstlane(tid >> 6);
  const int hh = lane >> 5, l31 = lane & 31, rsb = wid & 3, cg = wid >> 2;
  const int q4 = (lane & 15) >> 2, p4 = lane & 3, blk = (lane >> 4) & 1;
  const unsigned lds0 = (unsigned)(uintptr_t)lds;
  const lds_cptr L = (lds_cptr)(LAS char*)(uintptr_t)lds0;
  const int r8 = lane >> 3, c8 = lane & 7;
  const int xr0 = 16 * wid + r8;
  const char* xs = A.x + (size_t)(128 * rb + 16 * wid) * XROW;
  const unsigned xc0 = r8 * XROW + 16 * (c8 ^ ((xr0 >> 1) & 7)), xc1 = (r8 + 8) * XROW + 16 * (c8 ^ (((xr0 + 8) >> 1) & 7));
  const int sw0 = 32 * ((c8 >> 1) ^ (r8 & 3)) + 16 * (c8 & 1);
  const unsigned oq = r8 * QROW + sw0, og = r8 * FROW + sw0, ou = og ^ 32;
  const char* wq = A.wqkv + (size_t)(8 * wid) * QROW + 128 * cgp;
  const char* wg = A.wg + (size_t)(8 * wid) * FROW + 256 * cgp;
  const char* wu = A.wu + (size_t)(8 * wid) * FROW + 256 * cgp;
  auto pieceA = [&](int s, int p) {
    const unsigned sb = lds0 + posA(s);
    if (p == 0) glds16(xs + 128 * s, xc0, sb + 2048 * wid);
    if (p == 1) glds16(xs + 128 * s, xc1, sb + 2048 * wid + 1024);
    if (p == 2) glds16(wq + (size_t)(64 * s) * QROW, oq, sb + 16384 + 1024 * wid);
  };
  auto stageA = [&](int s) { pieceA(s, 0); pieceA(s, 1); pieceA(s, 2); };
  auto pieceU = [&](int u, int c, int p) {
    glds16((p ? wu : wg) + (size_t)(64 * c) * FROW + 128 * u, p ? ou : og, lds0 + c * 16384 + wid * 2048 + p * 1024);
  };
  STAMP_F(A.ws, 32);
  pieceA(0, 2);
  STAMP_F(A.ws, 33);
  unsigned tv0 = touch(A, wid, lane, 0), tv1 = touch(A, wid, lane, 1), tv2, tv3, genv;
  STAMP_F(A.ws, 34);
  stageA(1); stageA(2);
  STAMP_F(A.ws, 35);

  const int rt32 = rb * 4 + rsb;
  const int xrow = 32 * rsb + l31, xsw = (xrow >> 1) & 7;
  lds_cptr xb[4];
#pragma unroll
  for (int j = 0; j < 4; ++j) xb[j] = L + xrow * 128 + 16 * (((j << 1) | hh) ^ xsw);
  const lds_cptr wt0 = L + 16384 + (8 * hh + q4) * 128 + 8 * p4 + 32 * ((2 * cg + blk) ^ q4);
  const int rowb = hh * 2048 + blk * 1024 + q4 * 128 + 8 * p4;
  const lds_cptr wb0 = L + rowb + 32 * ((2 * cg) ^ q4 ^ blk), wb1 = L + rowb + 32 * ((2 * cg + 1) ^ q4 ^ blk);
  const bool isq = cgp < 7;
  const unsigned hs = isq ? 2048u : 4096u, pad = isq ? 0u : 0x3f80u;
  unsigned qo[2]; bool str[2];
#pragma unroll
  for (int t = 0; t < 2; ++t) {
    const int P = 8 * (isq ? cgp : cgp - 7) + 4 * cg + 2 * t + hh;
    const int hp = (P * 37) >> 8, r = P - 7 * hp;
    const int head = 2 * hp + (r > 3 ? 1 : 0), piece = r > 3 ? r - 4 : r;
    qo[t] = (isq ? WS_Q : WS_KV) + (head * 64 + rt32) * hs + l31 * 16 + piece * 512;
    str[t] = r == 3;
    PIN(qo[t]);
  }
  const float qs = isq ? QSCALE : 1.f;
  unsigned hbase = WS_H + (rt32 * 112 + 8 * cgp + 2 * cg) * 1024 + lane * 16;
  PIN(hbase);

  bf16x8 xf[28];
  f32x16 a0, a1, a2, b0, b1, zero16;
#pragma unroll
  for (int i = 0; i < 16; ++i) zero16[i] = 0.f;
  bf16x8 wf[4];
  lds_cptr cx[4], c0, c1;
  auto rdA = [&](auto I) {
    constexpr int i = decltype(I)::v, s = i >> 2, j = i & 3;
    if constexpr (j == 0) {
#pragma unroll
      for (int t = 0; t < 4; ++t) { cx[t] = xb[t] + posA(s); PIN(cx[t]); }
      c0 = wt0 + posA(s); PIN(c0);
    }
    xf[i] = ldsread16(cx[j]);
    wf[i & 1] = cat8(trread(c0 + j * 2048), trread(c0 + j * 2048 + 512));
  };
  auto rdG = [&](auto Q) {
    constexpr int q = decltype(Q)::v;
    if constexpr (q < 56) {
      constexpr int i = q >> 1;
      if constexpr (q % 28 == 0) { c0 = wb0 + i * 4096; c1 = wb1 + i * 4096; PIN(c0); PIN(c1); }
      const lds_cptr b = (q & 1) ? c1 : c0;
      wf[(q + 2) & 3] = cat8(trread(b + (i % 14) * 4096), trread(b + (i % 14) * 4096 + 512));
    } else if constexpr (q < 112) {
      constexpr int kk = (q - 56) % 28;
      if constexpr (kk % 14 == 0) { c0 = (q < 84 ? wb0 : wb1) + kk * 4096; PIN(c0); }
      wf[(q + 2) & 3] = cat8(trread(c0 + (kk % 14) * 4096), trread(c0 + (kk % 14) * 4096 + 512));
    }
  };

  STAMP_F(A.ws, 36);
  waitv_bar<8>();
  STAMP_AT(A.ws, 1);
  rdA(IC<0>{});
  sfor<0, 28>([&](auto I) {
    constexpr int i = decltype(I)::v, s = i >> 2;
    if constexpr ((i & 3) != 3) rdA(IC<i + 1>{});
    SBAR();
    a0 = MFMA32(wf[i & 1], xf[i], i == 0 ? zero16 : a0);
    if constexpr ((i & 3) == 3) {
      waitv_bar<(s < 3 ? 6 + s : s < 6 ? 9 : 10)>();
      STAMP_F(A.ws, 13 + 2 * s);
      if constexpr (s == 0) { PIN(tv0); PIN(tv1); }
      if constexpr (s == 6) { PIN(tv2); PIN(tv3); PIN(genv); }
      if constexpr (i < 27) rdA(IC<i + 1>{}); else { rdG(IC<0>{}); rdG(IC<1>{}); rdG(IC<2>{}); }
    }
    SBAR();
    if constexpr (i < 3) pieceA(3, i);
    else if constexpr (i < 15 && (i & 3) != 2) pieceA(4 + (i - 3) / 4, (i - 3) & 3);
    else if constexpr (i == 6) tv2 = touch(A, wid, lane, 2);
    else if constexpr (i == 10) tv3 = touch(A, wid, lane, 3);
    else if constexpr (i == 14) genv = load_gen(A);
    else if constexpr (i < 27) pieceU(0, (i - 15) >> 1, (i - 15) & 1);
    else { pieceU(0, 6, 0); pieceU(0, 6, 1); }
    SBAR();
  });
  STAMP_AT(A.ws, 2);
  const unsigned epoch = epoch_of(genv);
  u32x4 qk[2];
  float hv[8];
  auto poll = [&]() { glds16_dev(A.ws + F_QKV + 1024 * (blockIdx.x & 7), lane * 16, lds0 + RING0 + 2 * KVST + wid * 2048); };
  sfor<0, 112>([&](auto Q) {
    constexpr int q = decltype(Q)::v, i = q - 56;
    if constexpr (q < 64 && (q & 7) == 5) {
      constexpr int c = q >> 3;
      waitv_bar_nl<(c == 0 ? 12 : c == 1 ? 10 : c == 2 ? 10 : c == 3 ? 4 : c == 6 ? 2 : 20)>();
      if constexpr (c == 3) { STAMP_F(A.ws, 27); set_qkv_flag(rs, epoch); }
      if constexpr (c == 6) STAMP_F(A.ws, 29);
    }
    if constexpr (i == 13) waitv_bar_nl<3>();
    if constexpr (i == 21) waitv_bar_nl<2>();
    if constexpr (i == QKV_STEP) {
      if (wid == 0) {
        asm volatile("s_waitcnt vmcnt(0)" ::: "memory");
        const u32x4 f = __builtin_bit_cast(u32x4, ldsread16(L + RING0 + 2 * KVST + lane * 16));
        if (!__all(f[0] == epoch && f[1] == epoch && f[2] == epoch && f[3] == epoch)) wait_flags256(rs, F_QKV + 1024 * (blockIdx.x & 7), epoch, lane);
      }
      waitv_bar_nl<20>();
      STAMP_F(A.ws, 38);
    }
    rdG(IC<q + 3>{});
    SBAR();
    if constexpr (q < 56) {
      if constexpr (q & 1) a2 = MFMA32(wf[(q + 2) & 3], xf[q >> 1], q < 2 ? zero16 : a2);
      else a1 = MFMA32(wf[(q + 2) & 3], xf[q >> 1], q < 2 ? zero16 : a1);
    } else if constexpr (i < 28) b0 = MFMA32(wf[(q + 2) & 3], xf[i], i == 0 ? zero16 : b0);
    else b1 = MFMA32(wf[(q + 2) & 3], xf[i - 28], i == 28 ? zero16 : b1);
    SBAR();
    if constexpr (q >= 13 && q < 64 && ((q & 7) == 5 || (q & 7) == 6)) pieceU(1, (q >> 3) - 1, (q & 7) - 5);
    if constexpr (q == 0 || q == 2) {
      constexpr int t = q >> 1;
      const unsigned w0 = pk2(a0[8 * t] * qs, a0[8 * t + 1] * qs), w1 = pk2(a0[8 * t + 2] * qs, a0[8 * t + 3] * qs);
      const unsigned w2 = pk2(a0[8 * t + 4] * qs, a0[8 * t + 5] * qs), w3 = pk2(a0[8 * t + 6] * qs, a0[8 * t + 7] * qs);
      auto r0 = __builtin_amdgcn_permlane32_swap(w0, w2, false, false);
      auto r1 = __builtin_amdgcn_permlane32_swap(w1, w3, false, false);
      qk[t] = u32x4{r0[0], r1[0], r0[1], r1[1]};
      PIN(qk[t]);
    }
    if constexpr (q == 1 || q == 3) {
      constexpr int t = q >> 1;
      u32x4 v = qk[t];
      if (str[t]) { v[2] = pad; v[3] = 0u; }
      st16(rs, qo[t], v);
      if (str[t]) st16(rs, qo[t] + 64 * hs, u32x4{qk[t][2], qk[t][3], pad, 0u});
    }
    if constexpr (i >= 1 && i < 9) { hv[i - 1] = sigm_mul(a1[i - 1], a1[i + 7]); PIN(hv[i - 1]); }
    if constexpr (i == 9) st16(rs, hbase, pack8(hv));
    if constexpr (i >= 10 && i < 18) { hv[i - 10] = sigm_mul(a2[i - 10], a2[i - 2]); PIN(hv[i - 10]); }
    if constexpr (i == 18) st16(rs, hbase + 1024, pack8(hv));
    if constexpr (i >= 30 && i < 38) { hv[i - 30] = sigm_mul(b0[i - 30], b0[i - 22]); PIN(hv[i - 30]); }
    if constexpr (i == 38) { qk[0] = pack8(hv); PIN(qk[0]); }
    if constexpr (i == QKV_STEP + 6) { st16(rs, hbase + 4096, qk[0]); tvw = touch(A, wid, lane, 4); }
    if constexpr (i == QKV_STEP - 12) { if (wid == 0) poll(); }
    if constexpr (i >= QKV_STEP && i < QKV_STEP + 6) p2_first(A.ws, lds0, wid, lane * 16, i - QKV_STEP);
    SBAR();
  });
#pragma unroll
  for (int i = 0; i < 8; ++i) hv[i] = sigm_mul(b1[i], b1[i + 8]);
  st16(rs, hbase + 5120, pack8(hv));
  STAMP_AT(A.ws, 3);
  return epoch;
}

DI unsigned phase1_v(const Args& A, rsrc_t rs, char* lds, int rb, int cgp, unsigned& tvw STAMP_ARG) {
  const int tid = threadIdx.x, lane = tid & 63, wid = __builtin_amdgcn_readfirstlane(tid >> 6);
  const int hh = lane >> 5, l31 = lane & 31, rsb = wid & 3, cg = wid >> 2;
  const int q4 = (lane & 15) >> 2, p4 = lane & 3, blk = (lane >> 4) & 1;
  const unsigned lds0 = (unsigned)(uintptr_t)lds;
  const lds_cptr L = (lds_cptr)(LAS char*)(uintptr_t)lds0;
  const int r8 = lane >> 3, c8 = lane & 7;
  const int xr0 = 16 * wid + r8;
  const char* xs = A.x + (size_t)(128 * rb + 16 * wid) * XROW;
  const unsigned xc0 = r8 * XROW + 16 * (c8 ^ ((xr0 >> 1) & 7)), xc1 = (r8 + 8) * XROW + 16 * (c8 ^ (((xr0 + 8) >> 1) & 7));
  const unsigned oq = r8 * QROW + 32 * ((c8 >> 1) ^ (r8 & 3)) + 16 * (c8 & 1);
  const int l0 = cgp == 14 ? 14 : 18;
  const char* wa = A.wqkv + (size_t)(8 * wid) * QROW + 128 * l0;
  const char* wbs = A.wqkv + 128 * 17 + 64 * (cgp - 14);
  auto piece = [&](int s, int p) {
    if (s < 7) {
      const unsigned sb = lds0 + pos1(s);
      if (p == 0) glds16(xs + 128 * s, xc0, sb + 2048 * wid);
      if (p == 1) glds16(xs + 128 * s, xc1, sb + 2048 * wid + 1024);
      if (p >= 2) glds16(wa + (size_t)(64 * s) * QROW + 128 * (p - 2), oq, sb + 16384 + 8192 * (p - 2) + 1024 * wid);
    }
  };
  auto piece17 = [&](int p) {
    const int g = wid < 7 ? 4 * wid + p : 24 + p;
    glds16(wbs + (size_t)(16 * g) * QROW, (lane >> 2) * QROW + (lane & 3) * 16, lds0 + 122880 + g * 1024);
  };
#pragma unroll
  for (int p = 2; p < 5; ++p) piece(0, p);
  unsigned tv0 = touch(A, wid, lane, 0), tv1 = touch(A, wid, lane, 1), genv;
#pragma unroll
  for (int p = 0; p < 5; ++p) piece(1, p);
  const int rt32 = rb * 4 + rsb;
  const int xrow = 32 * rsb + l31, xsw = (xrow >> 1) & 7;
  lds_cptr xb[4];
#pragma unroll
  for (int j = 0; j < 4; ++j) xb[j] = L + xrow * 128 + 16 * (((j << 1) | hh) ^ xsw);
  const lds_cptr wt0 = L + 16384 + (8 * hh + q4) * 128 + 8 * p4 + 32 * ((2 * cg + blk) ^ q4);
  lds_cptr wb0 = L + 122880 + (8 * hh + q4) * 64 + 32 * blk + 8 * p4;
  PIN(wb0);
  unsigned vo[4];
#pragma unroll
  for (int t = 0; t < 4; ++t) {
    const int tile = t < 3 ? 2 * l0 + 2 * t + cg : 34 + cgp - 14;
    const int n = 32 * (tile - 28) + l31, head = (n * 2341) >> 16, dh = n - 28 * head;
    vo[t] = WS_KV + ((head * 64 + rt32) * 4 + 2) * 1024 + (dh + 32 * hh) * 16;
    PIN(vo[t]);
  }
  bf16x8 xf[28];
  f32x16 a0, a1, a2, b0, zero16;
#pragma unroll
  for (int i = 0; i < 16; ++i) zero16[i] = 0.f;
  bf16x8 wf[6];
  lds_cptr cx[4], c0;
  auto rdA = [&](auto I, auto M) {
    constexpr int i = decltype(I)::v, m = decltype(M)::v, s = i >> 2, j = i & 3, w = (i & 1) * 3;
    if constexpr (m == 0 && j == 0) {
#pragma unroll
      for (int t = 0; t < 4; ++t) { cx[t] = xb[t] + pos1(s); PIN(cx[t]); }
      c0 = wt0 + pos1(s); PIN(c0);
    }
    if constexpr (m == 0) xf[i] = ldsread16(cx[j]);
    wf[w + m] = cat8(trread(c0 + m * 8192 + j * 2048), trread(c0 + m * 8192 + j * 2048 + 512));
  };
  auto rdB = [&](auto I) {
    constexpr int i = decltype(I)::v;
    wf[i & 3] = cat8(trread(wb0 + i * 1024), trread(wb0 + i * 1024 + 256));
  };
  waitv_bar<7>();
  STAMP_AT(A.ws, 1);
  rdA(IC<0>{}, IC<0>{}); rdA(IC<0>{}, IC<1>{}); rdA(IC<0>{}, IC<2>{});
  sfor<0, 28>([&](auto I) {
    constexpr int i = decltype(I)::v, s = i >> 2, w = (i & 1) * 3;
    auto vm = [&](auto M) {
      constexpr int m = decltype(M)::v, r = i & 3;
      if constexpr (m == 0) piece(s + 2, r == 0 ? 0 : r + 1);
      if constexpr (m == 1 && r == 0) piece(s + 2, 1);
      if constexpr (i == 11 && m == 1) genv = load_gen(A);
      if constexpr (i >= 20 && i < 22 && m < 2) piece17(2 * (i - 20) + m);
      if constexpr (i == 22 && m == 0) glds16(A.gw, (lane < 56 ? lane : 55) * 16, lds0 + 151552 + 1024 * wid);
    };
    auto rd = [&](auto M) { if constexpr (i < 27) rdA(IC<i + 1>{}, M); else rdB(M); };
    SBAR();
    a0 = MFMA32(xf[i], wf[w], i == 0 ? zero16 : a0);
    if constexpr ((i & 3) == 3) {
      waitv_bar<(s == 3 ? 5 : s == 5 ? 5 : s == 6 ? 0 : 4)>();
      STAMP_F(A.ws, 13 + 2 * s);
      if constexpr (s == 0) { PIN(tv0); PIN(tv1); }
      if constexpr (s == 4) PIN(genv);
    }
    SBAR(); rd(IC<0>{}); vm(IC<0>{}); SBAR();
    a1 = MFMA32(xf[i], wf[w + 1], i == 0 ? zero16 : a1);
    SBAR(); rd(IC<1>{}); vm(IC<1>{}); SBAR();
    a2 = MFMA32(xf[i], wf[w + 2], i == 0 ? zero16 : a2);
    SBAR(); rd(IC<2>{}); vm(IC<2>{}); SBAR();
  });
  STAMP_AT(A.ws, 2);
  const unsigned epoch = epoch_of(genv);
  auto vstore = [&](const f32x16& a, unsigned o) {
    float v[16];
#pragma unroll
    for (int i = 0; i < 16; ++i) v[i] = a[i];
    st16(rs, o, pack8(v)); st16(rs, o + 1024, pack8(v + 8));
  };
  {
    const int head = 8 * (cgp - 14) + 4 * cg + (lane >> 4);
    const unsigned o = WS_KV + ((head * 64 + rt32) * 4 + 2 + ((lane >> 3) & 1)) * 1024 + (28 + (lane & 3) + 32 * ((lane >> 2) & 1)) * 16;
    const unsigned v = (lane & 3) == 0 ? 0x3f803f80u : 0u;
    st16(rs, o, u32x4{v, v, v, v});
  }
  if (cg == 0) {
    sfor<0, 28>([&](auto I) {
      constexpr int i = decltype(I)::v;
      if constexpr (i + 3 < 28) rdB(IC<i + 3>{});
      SBAR();
      b0 = MFMA32(xf[i], wf[i & 3], i == 0 ? zero16 : b0);
      if constexpr (i == 2) vstore(a0, vo[0]);
      if constexpr (i == 5) vstore(a1, vo[1]);
      if constexpr (i == 9) vstore(a2, vo[2]);
      SBAR();
    });
    vstore(b0, vo[3]);
    STAMP_F(A.ws, 27);
    waitv_bar<0>();
    STAMP_F(A.ws, 29);
    set_qkv_flag(rs, epoch);
  } else {
    vstore(a0, vo[0]); vstore(a1, vo[1]); vstore(a2, vo[2]);
    waitv_bar<0>();
    if (cgp == 14) {
      const lds_cptr gp = L + 151552 + 1024 * wid + 16 * hh;
      f32x16 ga = zero16;
#pragma unroll
      for (int i = 0; i < 28; ++i) ga = MFMA32(ldsread16(gp + 32 * i), xf[i], ga);
      const float g = rbf(1.f / (1.f + __expf(-rbf(ga[0]))));
      if (lane < 32) st4(rs, WS_G + 4 * (32 * rt32 + lane), __float_as_uint(g));
    }
  }
  tvw = touch(A, wid, lane, 4);
  STAMP_AT(A.ws, 3);
  if (wid == 0) wait_flags256(rs, F_QKV + 1024 * (blockIdx.x & 7), epoch, lane);
  STAMP_F(A.ws, 38);
  __syncthreads();
#pragma unroll
  for (int j = 0; j < 6; ++j) p2_first(A.ws, lds0, wid, lane * 16, j);
  return epoch;
}

DI float xhalf_max(float v) {
  auto r = __builtin_amdgcn_permlane32_swap(__float_as_uint(v), __float_as_uint(v), false, false);
  return fmaxf(__uint_as_float(r[0]), __uint_as_float(r[1]));
}
DI float max16(const f32x16& s) {
  float a = fmaxf(fmaxf(s[0], s[1]), s[2]), b = fmaxf(fmaxf(s[3], s[4]), s[5]);
  a = fmaxf(fmaxf(a, s[6]), s[7]); b = fmaxf(fmaxf(b, s[8]), s[9]);
  a = fmaxf(fmaxf(a, s[10]), s[11]); b = fmaxf(fmaxf(b, s[12]), s[13]);
  a = fmaxf(fmaxf(a, s[14]), s[15]);
  return fmaxf(a, b);
}

DI void phase23(const Args& A, rsrc_t rs, char* lds, int rb1, int cgp, unsigned epoch, unsigned tvw STAMP_ARG) {
  const int bid = blockIdx.x, xcd = bid & 7, idx = bid >> 3;
  const int hd = 2 * xcd + (idx & 1), rb = idx >> 1;
  const int i3 = idx < 28 ? idx : idx - 28, rt64 = 4 * xcd + i3 / 7, c7 = i3 % 7;
  const int tid = threadIdx.x, lane = tid & 63, wid = __builtin_amdgcn_readfirstlane(tid >> 6);
  const int hh = lane >> 5, qs = wid & 3, kh = wid >> 2;
  const unsigned lds0 = (unsigned)(uintptr_t)lds;
  const lds_cptr L = (lds_cptr)(LAS char*)(uintptr_t)lds0;
  const unsigned kvo = WS_KV + hd * (64 * 4096) + kh * (32 * 4096);
  const unsigned l16 = lane * 16;
  auto stage = [&](int s, int slot, int p) { kv_stage(A.ws, lds0, wid, l16, s, slot, p); };
  const int r3 = wid & 1, kq = wid >> 1;
  const int r8 = lane >> 3, c8 = lane & 7;
  const unsigned ow = r8 * XROW + 32 * ((c8 >> 1) ^ (r8 & 3)) + 16 * (c8 & 1);
  const char* wds = A.wd + (size_t)(448 * kq + 16 * r3) * XROW + 128 * c7;
  auto stage_wd = [&](int c, int slot, int p) {
    glds16(wds + (size_t)(32 * c + 8 * p) * XROW, ow, lds0 + slot * 16384 + wid * 2048 + p * 1024);
  };

  waitv_bar<4>();
  STAMP_AT(A.ws, 4);
  const lds_cptr pb = L + RING0 + 2 * KVST + l16;
  const bf16x8 q0 = ldsread16(pb + 8192 + qs * 2048), q1 = ldsread16(pb + 8192 + qs * 2048 + 1024);
  const bf16x8 ka0 = ldsread16(pb + kh * 4096), ka1 = ldsread16(pb + kh * 4096 + 1024);
  bf16x8 kf0 = ldsread16(pb + kh * 4096 + 2048), kf1 = ldsread16(pb + kh * 4096 + 3072);
  f32x16 zero16;
#pragma unroll
  for (int i = 0; i < 16; ++i) zero16[i] = 0.f;
  f32x16 sA = MFMA32(ka0, q0, zero16);
  float mref;
  {
    f32x16 s = MFMA32(ka1, q1, sA);
    mref = rbf(xhalf_max(max16(s)));
  }
  bf16x8 q1m = q1;
  if (hh) q1m[4] = (short)((__float_as_uint(-mref)) >> 16);
  sA = MFMA32(ka1, q1m, sA);
  PIN(kf0); PIN(kf1);
  STAMP_AT(A.ws, 5);
  f32x16 o = zero16, sB;
  bf16x8 vf0, vf1;
  u32x4 p0 = {0, 0, 0, 0}, p1 = {0, 0, 0, 0};
  float e0 = 0.f, e1 = 0.f, e2 = 0.f, e3 = 0.f;
#pragma unroll
  for (int i = 0; i < 8; ++i) vf1[i] = 0;
  lds_cptr rb0 = L + RING0 + kh * 8192 + lane * 16;
  PIN(rb0);
  bf16x8 hf[28];
  unsigned fh;
  float g;
  const unsigned ho = WS_H + ((rt64 * 2 + r3) * 112 + 28 * kq) * 1024 + lane * 16;
#define G(MF, RDS, W0, W1, C, i) do { MF; SBAR(); RDS; SBAR(); \
    float n0 = EX2(C[i]); PIN(n0); SBAR(); W0 = pk2(e0, e1); PIN(W0); SBAR(); float n1 = EX2(C[i + 1]); PIN(n1); SBAR(); W1 = pk2(e2, e3); PIN(W1); SBAR(); \
    float n2 = EX2(C[i + 2]); PIN(n2); float n3 = EX2(C[i + 3]); PIN(n3); e0 = n0; e1 = n1; e2 = n2; e3 = n3; SBAR(); } while (0)
#define STEP(C, N, b, PA, PB, X0, X1, X2) do { \
    __builtin_amdgcn_s_setprio(PA); \
    G(N = MFMA32(kf0, q0, zero16), X0, p1[2], p1[3], C, 0); \
    G(o = MFMA32(vf1, __builtin_bit_cast(bf16x8, p1), o), vf0 = ldsread16((b) + 2048); X1, p0[0], p0[1], C, 4); \
    __builtin_amdgcn_s_setprio(PB); \
    G(N = MFMA32(kf1, q1m, N), vf1 = ldsread16((b) + 3072); kf0 = ldsread16((b)); kf1 = ldsread16((b) + 1024), p0[2], p0[3], C, 8); \
    G(o = MFMA32(vf0, __builtin_bit_cast(bf16x8, p0), o), X2, p1[0], p1[1], C, 12); \
  } while (0)
  sfor<0, 16>([&](auto S) {
    constexpr int s = decltype(S)::v;
    constexpr auto E = [](int t) { return t < 0 ? 0 : t < 7 ? 2 : (t >= 8 && t < 15) ? 4 : 0; };
    constexpr int N = (s + 1 < 16 ? 2 : 0) + E(s - 2) + E(s - 1);
    if constexpr (s == 8) {
      PIN(fh);
      if (wid == 0 && !__all(fh == epoch)) wait_flags(rs, F_H + 128 * (rt64 >> 1), 16, epoch, lane);
    }
    waitv_bar<N>();
    if constexpr (s == 1) PIN(tvw);
    if constexpr (s == 1) { if (tid == 0) st4(rs, F_H + 128 * rb1 + 4 * cgp, epoch); }
    if constexpr (s == 5) fh = look16(A.ws + F_H + 128 * (rt64 >> 1), wid, lane);
    auto vm = [&](auto J) {
      constexpr int j = decltype(J)::v;
      if constexpr (j < 2) { if constexpr (s + 2 < 16) stage(s + 2, (s + 2) % 3, j); }
      else if constexpr (s < 7) { if constexpr (j < 4) stage_wd(s, s, j - 2); }
      else if constexpr (s >= 8 && s < 15) hf[4 * (s - 8) + j - 2] = ldfrag(rs, ho + (4 * (s - 8) + j - 2) * 1024);
      else if constexpr (s == 15 && j == 2) g = __uint_as_float(ld4(rs, WS_G + 4 * (rt64 * 64 + r3 * 32 + (lane & 31))));
    };
    SBAR();
    const lds_cptr b = rb0 + (s % 3) * KVST;
    STEP(sA, sB, b, 3, 2, vm(IC<0>{}), vm(IC<1>{}), vm(IC<2>{}));
    STEP(sB, sA, b + 4096, 1, 0, vm(IC<3>{}), vm(IC<4>{}), vm(IC<5>{}));
  });
  p1[2] = pk2(e0, e1); p1[3] = pk2(e2, e3);
  o = MFMA32(vf1, __builtin_bit_cast(bf16x8, p1), o);
#undef STEP
#undef G
  STAMP_AT(A.ws, 6);
  {
    bool bad = false;
#pragma unroll
    for (int i = 0; i < 13; ++i) bad |= !(fabsf(o[i]) <= 1.0e30f);
#pragma unroll
    for (int i = 13; i < 16; ++i) bad |= !hh && !(fabsf(o[i]) <= 1.0e30f);
    if (__any(bad)) {
      float m = -INFINITY;
      o = zero16;
#pragma unroll 1
      for (int t = 0; t < 32; ++t) {
        const unsigned p = kvo + t * 4096 + lane * 16;
        f32x16 s = MFMA32(ldfrag(rs, p), q0, zero16);
        s = MFMA32(ldfrag(rs, p + 1024), q1, s);
        const float mn = fmaxf(m, xhalf_max(max16(s)));
        const float al = EX2(m - mn);
        m = mn;
        float e[16];
#pragma unroll
        for (int i = 0; i < 16; ++i) { o[i] *= al; e[i] = EX2(s[i] - mn); }
        o = MFMA32(ldfrag(rs, p + 2048), __builtin_bit_cast(bf16x8, pack8(e)), o);
        o = MFMA32(ldfrag(rs, p + 3072), __builtin_bit_cast(bf16x8, pack8(e + 8)), o);
      }
      mref = m;
    }
  }
  {
    auto lr = __builtin_amdgcn_permlane32_swap(__float_as_uint(o[12]), __float_as_uint(o[12]), false, false);
    const float lown = __uint_as_float(lr[1]);
    f32x4* xs = (f32x4*)(lds + RING0 + KVST) + wid * (3 * 64) + lane;
    const f32x4* xr = (const f32x4*)(lds + RING0 + KVST) + (wid ^ 4) * (3 * 64) + lane;
    f32x4 k0, k1;
    if (kh == 0) { xs[0] = f32x4{o[8], o[9], o[10], o[11]}; xs[64] = f32x4{o[12], o[13], o[14], o[15]}; k0 = f32x4{o[0], o[1], o[2], o[3]}; k1 = f32x4{o[4], o[5], o[6], o[7]}; }
    else { xs[0] = f32x4{o[0], o[1], o[2], o[3]}; xs[64] = f32x4{o[4], o[5], o[6], o[7]}; k0 = f32x4{o[8], o[9], o[10], o[11]}; k1 = f32x4{o[12], o[13], o[14], o[15]}; }
    xs[128] = f32x4{mref, lown, 0.f, 0.f};
    waitv_bar<0>();
    PIN(g);
    const f32x4 t0 = xr[0], t1 = xr[64], ml = xr[128];
    const float M = fmaxf(mref, ml[0]);
    float a0 = EX2(mref - M), a1 = EX2(ml[0] - M);
    const float inv = 1.f / (lown * a0 + ml[1] * a1);
    a0 *= inv; a1 *= inv;
    float v[8];
#pragma unroll
    for (int j = 0; j < 4; ++j) { v[j] = k0[j] * a0 + t0[j] * a1; v[4 + j] = k1[j] * a0 + t1[j] * a1; }
    if (kh && hh) { v[4] = 0.f; v[5] = 0.f; v[6] = 0.f; v[7] = 0.f; }
    st16(rs, WS_O + (((rb * 4 + qs) * 16 + hd) * 2 + kh) * 1024 + l16, pack8(v));
  }
  STAMP_AT(A.ws, 7);
  if (idx >= 28) {
    waitv_bar<0>();
    if (tid == 0) st4(rs, F_O + 128 * rb + 4 * hd, epoch);
    if (bid == 255 && tid == 0) st4(rs, F_GEN, epoch);
    return;
  }

  const int q4 = (lane & 15) >> 2, p4 = lane & 3, blk = (lane >> 4) & 1;
  const int wro = (4 * hh + q4) * 128 + 8 * p4;
  const lds_cptr wd0 = L + kq * 4096 + wro + 32 * ((0 + blk) ^ q4), wd1 = L + kq * 4096 + wro + 32 * ((2 + blk) ^ q4);
  f32x16 bc0, bc1;
  bf16x8 wf[4], of[8];
  lds_cptr d0, d1;
  auto rdW = [&](auto Q) {
    constexpr int q = decltype(Q)::v, sl = (q >> 2) % 10, so = (sl % 3) * 16384 + ((q >> 1) & 1) * 2048;
    if constexpr (q < 56) {
      if constexpr ((q & 3) == 0 && sl % 3 == 0) { d0 = wd0 + sl * 16384; d1 = wd1 + sl * 16384; PIN(d0); PIN(d1); }
      const lds_cptr b = (q & 1) ? d1 : d0;
      wf[q & 3] = cat8(trread(b + so), trread(b + so + 1024));
    }
  };
  const char* wos = A.wo + (size_t)(56 * wid) * XROW + 128 * c7;
  const unsigned oo = WS_O + (((rt64 * 2 + r3) * 16 + 4 * kq) * 2) * 1024 + l16;
  unsigned fo;
  rdW(IC<0>{}); rdW(IC<1>{}); rdW(IC<2>{});
  sfor<0, 56>([&](auto Q) {
    constexpr int q = decltype(Q)::v;
    if constexpr (q == 2) waitv_bar_nl<20>();
    if constexpr (q == 18) {
      waitv_bar_nl<6>();
      if (tid == 0) st4(rs, F_O + 128 * rb + 4 * hd, epoch);
    }
    if constexpr (q == 25) waitv_bar_nl<7>();
    if constexpr (q == 34) waitv_bar_nl<20>();
    if constexpr (q == 37) waitv_bar_nl<4>();
    if constexpr (q == O_STEP) {
      STAMP_AT(A.ws, 8);
      asm volatile("s_waitcnt vmcnt(7)" : "+v"(fo)::"memory");
      if (wid == 0 && !__all(fo == epoch)) wait_flags(rs, F_O + 128 * (rt64 >> 1), 16, epoch, lane);
      waitv_bar_nl<20>();
      STAMP_AT(A.ws, 9);
    }
    rdW(IC<q + 3>{});
    SBAR();
    if constexpr (q & 1) bc1 = MFMA32(wf[q & 3], hf[q >> 1], q < 2 ? zero16 : bc1);
    else bc0 = MFMA32(wf[q & 3], hf[q >> 1], q < 2 ? zero16 : bc0);
    SBAR();
    if constexpr (q >= 2 && q < 8) stage_wd(7 + (q - 2) / 2, 7 + (q - 2) / 2, q & 1);
    if constexpr (q >= 18 && q < 26) stage_wd(10 + (q - 18) / 2, (q - 18) / 2, q & 1);
    if constexpr (q == 33) fo = look16(A.ws + F_O + 128 * (rt64 >> 1), wid, lane);
    if constexpr (q >= 34 && q < 41) glds16(wos + (size_t)(8 * (q - 34)) * XROW, ow, lds0 + 65536 + wid * 7168 + (q - 34) * 1024);
    if constexpr (q >= O_STEP && q < O_STEP + 8) of[q - O_STEP] = ldfrag(rs, oo + (q - O_STEP) * 1024);
    SBAR();
  });
  const float gm = rbf(1.f - g);
#pragma unroll
  for (int i = 0; i < 16; ++i) { bc0[i] *= gm; bc1[i] *= gm; }
  waitv_bar<8>();
  STAMP_AT(A.ws, 10);
  f32x16 ac0 = zero16, ac1 = zero16;
  {
    lds_cptr wo0 = L + 65536 + kq * (112 * 128) + wro + 32 * ((0 + blk) ^ q4), wo1 = L + 65536 + kq * (112 * 128) + wro + 32 * ((2 + blk) ^ q4);
    lds_cptr wo0h = wo0 + (hh ? 2048 : 3072), wo1h = wo1 + (hh ? 2048 : 3072);
    PIN(wo0); PIN(wo1); PIN(wo0h); PIN(wo1h);
#pragma unroll
    for (int h = 0; h < 4; ++h) {
      const int b = h * 28 * 128;
      ac0 = MFMA32(cat8(trread(wo0 + b), trread(wo0 + b + 1024)), of[2 * h], ac0);
      ac1 = MFMA32(cat8(trread(wo1 + b), trread(wo1 + b + 1024)), of[2 * h], ac1);
      ac0 = MFMA32(cat8(trread(wo0 + b + 2048), trread(wo0h + b)), of[2 * h + 1], ac0);
      ac1 = MFMA32(cat8(trread(wo1 + b + 2048), trread(wo1h + b)), of[2 * h + 1], ac1);
    }
  }
  {
    char* rp = lds + (kq < 3 ? kq * (64 * 272) : 122880) + (r3 * 32 + (lane & 31)) * 272 + 16 * hh;
#pragma unroll
    for (int i = 0; i < 4; ++i) {
      *(f32x4*)(rp + 32 * i) = f32x4{g * ac0[4 * i] + bc0[4 * i], g * ac0[4 * i + 1] + bc0[4 * i + 1], g * ac0[4 * i + 2] + bc0[4 * i + 2], g * ac0[4 * i + 3] + bc0[4 * i + 3]};
      *(f32x4*)(rp + 128 + 32 * i) = f32x4{g * ac1[4 * i] + bc1[4 * i], g * ac1[4 * i + 1] + bc1[4 * i + 1], g * ac1[4 * i + 2] + bc1[4 * i + 2], g * ac1[4 * i + 3] + bc1[4 * i + 3]};
    }
  }
  __syncthreads();
  {
    const int row = tid >> 3, cc = tid & 7;
    const char* rp = lds + row * 272 + cc * 32;
    f32x4 s0 = *(const f32x4*)rp, s1 = *(const f32x4*)(rp + 16);
#pragma unroll
    for (int k = 1; k < 4; ++k) { const int ko = k < 3 ? k * (64 * 272) : 122880; s0 += *(const f32x4*)(rp + ko); s1 += *(const f32x4*)(rp + ko + 16); }
    u32x4 w = {pk2(s0[0], s0[1]), pk2(s0[2], s0[3]), pk2(s1[0], s1[1]), pk2(s1[2], s1[3])};
    *(u32x4*)(A.out + (size_t)(rt64 * 64 + row) * XROW + c7 * 128 + cc * 16) = w;
  }
  STAMP_AT(A.ws, 11);
  asm volatile("fused_code_end:");
}

__global__ __launch_bounds__(512, 2) void fused_kernel(Args A) {
  extern __shared__ __attribute__((aligned(16))) char lds[];
  const int bid = blockIdx.x, xcd = bid & 7, idx = bid >> 3;
  const int rb1 = 4 * (xcd & 3) + (idx & 3), cgp = idx < 28 ? 7 * (xcd >> 2) + (idx >> 2) : 14 + (xcd >> 2);
  STAMP_AT(A.ws, 0);
  {
    const int lane = threadIdx.x & 63, wid = __builtin_amdgcn_readfirstlane(threadIdx.x >> 6);
    const int r8 = lane >> 3, c8 = lane & 7, xr0 = 16 * wid + r8;
    const char* xs = A.x + (size_t)(128 * rb1 + 16 * wid) * XROW;
    const unsigned sb = (unsigned)(uintptr_t)lds + (cgp < 14 ? posA(0) : pos1(0)) + 2048 * wid;
    glds16(xs, r8 * XROW + 16 * (c8 ^ ((xr0 >> 1) & 7)), sb);
    glds16(xs, (r8 + 8) * XROW + 16 * (c8 ^ (((xr0 + 8) >> 1) & 7)), sb + 1024);
  }
  unsigned cv = 0;
  {
    unsigned long long pc; unsigned len;
    asm volatile("s_getpc_b64 %0\nfused_code_begin:\n\ts_mov_b32 %1, fused_code_end-fused_code_begin" : "=s"(pc), "=s"(len));
    const unsigned off = (idx * 16 + (threadIdx.x & 15)) * 128;
    if (threadIdx.x < 16 && off < len) asm volatile("global_load_dword %0, %1, %2" : "=v"(cv) : "v"(off), "s"(pc) : "memory");
  }
  const rsrc_t rs = __builtin_amdgcn_make_buffer_rsrc((void*)A.ws, 0, WS_END, 0x00020000);
  unsigned epoch;
  unsigned tvw = 0;
  if (cgp < 14) epoch = phase1_gu(A, rs, lds, rb1, cgp, tvw STAMP_PASS);
  else epoch = phase1_v(A, rs, lds, rb1, cgp, tvw STAMP_PASS);
  PIN(cv);
  phase23(A, rs, lds, rb1, cgp, epoch, tvw STAMP_PASS);
  STAMP_FLUSH(A.ws);
}

extern "C" void kernel_launch(void* const* d_in, const int* in_sizes, int n_in, void* d_out, int out_size, void* d_ws, size_t ws_size,
                              hipStream_t stream) {
  Args A;
  A.x = (const char*)d_in[0]; A.wqkv = (const char*)d_in[1]; A.wo = (const char*)d_in[2];
  A.wg = (const char*)d_in[4]; A.wu = (const char*)d_in[5]; A.wd = (const char*)d_in[6]; A.gw = (const char*)d_in[7];
  A.ws = (char*)d_ws; A.out = (char*)d_out;
  (void)hipFuncSetAttribute((const void*)fused_kernel, hipFuncAttributeMaxDynamicSharedMemorySize, LDS_BYTES);
  hipLaunchKernelGGL(fused_kernel, dim3(256), dim3(512), LDS_BYTES, stream, A);
}
```

```cpp
#include <hip/hip_runtime.h>
#include <hip/hip_bf16.h>
#include <stdint.h>

typedef __attribute__((ext_vector_type(8))) short bf16x8;
typedef __attribute__((ext_vector_type(4))) short s16x4;
typedef __attribute__((ext_vector_type(16))) float f32x16;
typedef __attribute__((ext_vector_type(4))) float f32x4;
typedef __attribute__((ext_vector_type(4))) unsigned u32x4;
typedef __attribute__((ext_vector_type(2))) unsigned u32x2;
typedef __attribute__((ext_vector_type(2))) float f32x2;
typedef __attribute__((ext_vector_type(2))) __bf16 bf16x2_t;
#define LAS __attribute__((address_space(3)))
#define GAS __attribute__((address_space(1)))
typedef LAS const char* lds_cptr;
typedef LAS char* lds_ptr;
typedef __amdgpu_buffer_rsrc_t rsrc_t;

#define DI __device__ __forceinline__
#define MFMA32(a, b, c) __builtin_amdgcn_mfma_f32_32x32x16_bf16((a), (b), (c), 0, 0, 0)
#define SBAR() __builtin_amdgcn_sched_barrier(0)
#define PIN(x) asm volatile("" : "+v"(x))
#define EX2(x) __builtin_amdgcn_exp2f(x)

#define STAMP_F(ws, slot) do {} while (0)
#define STAMP_ARG
#define STAMP_PASS
#define STAMP_AT(ws, slot) do {} while (0)
#define STAMP_FLUSH(ws) do {} while (0)

constexpr int D_ = 448, F_ = 1792;
constexpr int XROW = D_ * 2, QROW = 3 * D_ * 2, FROW = F_ * 2;
constexpr float QSCALE = 0.18898223650461362f * 1.4426950408889634f;
constexpr float LOG2E = 1.4426950408889634f;

constexpr unsigned WS_Q = 0;
constexpr unsigned WS_KV = WS_Q + (2u << 20);
constexpr unsigned WS_H = WS_KV + (4u << 20);
constexpr unsigned WS_O = WS_H + (7u << 20);
constexpr unsigned WS_G = WS_O + (2u << 20);
constexpr unsigned WS_F = WS_G + 8192;
constexpr unsigned F_GEN = WS_F, F_H = WS_F + 4096, F_O = WS_F + 8192, F_QKV = WS_F + 16384;
#define NREP 32
constexpr unsigned WS_END = WS_F + 16384 + 65536;

template <int I> struct IC { static constexpr int v = I; };
template <int B, int E, class Fn> DI void sfor(Fn&& f) {
  if constexpr (B < E) { f(IC<B>{}); sfor<B + 1, E>(f); }
}

DI unsigned pk2(float lo, float hi) {
  f32x2 v = {lo, hi};
  bf16x2_t r = __builtin_convertvector(v, bf16x2_t);
  return __builtin_bit_cast(unsigned, r);
}
DI float rbf(float x) { return __uint_as_float(pk2(x, 0.f) << 16); }
DI u32x4 pack8(const float* v) { return u32x4{pk2(v[0], v[1]), pk2(v[2], v[3]), pk2(v[4], v[5]), pk2(v[6], v[7])}; }

DI void glds16(const char* base, unsigned voff, unsigned lds_base) {
  asm volatile("s_mov_b32 m0, %2\n\ts_nop 0\n\tglobal_load_lds_dwordx4 %0, %1" ::"v"(voff), "s"(base), "s"(lds_base) : "memory");
}
DI void glds16_dev(const char* base, unsigned voff, unsigned lds_base) {
  asm volatile("s_mov_b32 m0, %2\n\ts_nop 0\n\tglobal_load_lds_dwordx4 %0, %1 sc1" ::"v"(voff), "s"(base), "s"(lds_base) : "memory");
}
typedef short v4i16_t __attribute__((ext_vector_type(4)));
DI s16x4 trread(lds_cptr p) {
  return __builtin_bit_cast(s16x4, __builtin_amdgcn_ds_read_tr16_b64_v4i16((LAS v4i16_t*)p));
}
DI bf16x8 ldsread16(lds_cptr p) { return *(const LAS bf16x8*)p; }
DI bf16x8 cat8(s16x4 a, s16x4 b) { return __builtin_shufflevector(a, b, 0, 1, 2, 3, 4, 5, 6, 7); }
template <int N> DI void waitv_bar() {
  SBAR();
  asm volatile("s_waitcnt vmcnt(%0) lgkmcnt(0)\n\ts_barrier" ::"i"(N) : "memory");
  SBAR();
}
template <int N> DI void waitv_bar_nl() {
  SBAR();
  asm volatile("s_waitcnt vmcnt(%0)\n\ts_barrier" ::"i"(N) : "memory");
  SBAR();
}
DI void st16(rsrc_t r, unsigned off, u32x4 v) { __builtin_amdgcn_raw_buffer_store_b128(v, r, off, 0, 16); }
DI void st4(rsrc_t r, unsigned off, unsigned v) { __builtin_amdgcn_raw_buffer_store_b32(v, r, off, 0, 16); }
DI u32x4 ld16(rsrc_t r, unsigned off) { return __builtin_amdgcn_raw_buffer_load_b128(r, off, 0, 16); }
DI unsigned ld4(rsrc_t r, unsigned off) { return __builtin_amdgcn_raw_buffer_load_b32(r, off, 0, 16); }
DI bf16x8 ldfrag(rsrc_t r, unsigned off) { return __builtin_bit_cast(bf16x8, ld16(r, off)); }

struct Args {
  const char *x, *wqkv, *wo, *wg, *wu, *wd, *gw;
  char* ws;
  char* out;
};

constexpr int LDS_BYTES = 163840;
constexpr int SLOT = 40960;
DI constexpr int pos1(int s) { return (s % 3) * SLOT; }

#define QSTEP 30
constexpr int QKV_STEP = QSTEP, O_STEP = 44;
constexpr int SPIN_CAP = 100000;
DI void wait_flags(rsrc_t rs, unsigned off, int n, unsigned epoch, int lane) {
  const unsigned o = off + 4 * (lane < n ? lane : 0);
  for (int spin = 0; spin < SPIN_CAP; ++spin) {
    const unsigned v = ld4(rs, o);
    if (__all(v == epoch)) break;
    __builtin_amdgcn_s_sleep(4);
  }
  asm volatile("" ::: "memory");
}
DI void wait_flags256(rsrc_t rs, unsigned off, unsigned epoch, int lane) {
  bool ok = lane >= 56;
  for (int spin = 0; spin < SPIN_CAP; ++spin) {
    const u32x4 v = ld16(rs, off + 16 * (ok ? 55 : lane));
    ok = ok || (v[0] == epoch && v[1] == epoch && v[2] == epoch && v[3] == epoch);
    if (__all(ok)) break;
    __builtin_amdgcn_s_sleep(8);
  }
  asm volatile("" ::: "memory");
}

DI unsigned look16(const char* p, int wid, int lane, int mask = 15) {
  unsigned v = 0;
  asm volatile("s_cmp_eq_u32 %3, 0\n\ts_cselect_b64 exec, -1, 0\n\tglobal_load_dword %0, %1, %2 sc1\n\ts_mov_b64 exec, -1"
               : "+v"(v) : "v"(4 * (lane & mask)), "s"(p), "s"(wid) : "memory", "scc");
  return v;
}
DI float sigm_mul(float g, float u) { return u * __builtin_amdgcn_rcpf(1.f + EX2(-LOG2E * g)); }

constexpr int RING0 = 114688, KRING = RING0, VRING = RING0 + 24576, VX = 90112, VLAG = 6;
struct KV { const char* kvb; const char* pre; unsigned dst; int lt0; };
DI KV kv_setup(const char* ws, unsigned lds0, int wid) {
  const int bid = blockIdx.x, idx = bid >> 3, hd = 2 * (bid & 7) + (idx & 1), rb = idx >> 1;
  KV k;
  k.kvb = ws + WS_KV + hd * (64 * 4096) + (wid >> 2) * (32 * 4096) + (wid & 1) * 1024;
  k.lt0 = ((wid >> 1) & 1) + 2 * rb;
  k.dst = lds0 + wid * 1024;
  k.pre = ws + (wid < 4 ? WS_KV + hd * (64 * 4096) + (wid >> 1) * (32 * 4096) + (((wid & 1) + 2 * rb) & 31) * 4096 : WS_Q + (hd * 64 + rb * 4 + (wid - 4)) * 2048);
  asm volatile("" : "+s"(k.kvb), "+s"(k.pre), "+s"(k.dst), "+s"(k.lt0));
  return k;
}
DI constexpr int vpos(int u) { return u < 3 ? VX + u * 8192 : VRING + (u % 3) * 8192; }
DI void k_unit(const KV& k, unsigned l16, int s) { glds16_dev(k.kvb + (((2 * s + 2 + k.lt0) & 31) << 12), l16, k.dst + KRING + (s % 3) * 8192); }
DI void v_unit(const KV& k, unsigned l16, int u) { glds16_dev(k.kvb + (((2 * u + k.lt0) & 31) << 12) + 2048, l16, k.dst + vpos(u)); }
DI void p2_first(const KV& k, unsigned lds0, int wid, unsigned l16, int j) {
  if (j >= 2) k_unit(k, l16, j - 2);
  else glds16_dev(k.pre + j * 1024, l16, lds0 + VRING + wid * 2048 + j * 1024);
}
DI void set_qkv_flag(rsrc_t rs, unsigned epoch) {
  if (threadIdx.x < NREP) st4(rs, F_QKV + 1024 * threadIdx.x + 4 * blockIdx.x, epoch);
}

DI unsigned touch(const Args& A, int wid, int lane, int r) {
  const int w = blockIdx.x * 8 + wid;
  unsigned v;
  if (r == 0) {
    const int row = (w - 1792) * 2 + ((lane >> 3) & 1), l = lane & 7;
    asm volatile("global_load_dword %0, %1, %2" : "=v"(v) : "v"((row < 448 ? row : 447) * QROW + (14 + (l < 7 ? l : 6)) * 128), "s"(A.wqkv) : "memory");
  } else {
    const char* p = w < 1568 ? A.wd + (size_t)w * 1024 : A.wo + (size_t)(w < 1960 ? w - 1568 : w - 1960) * 1024;
    asm volatile("global_load_dword %0, %1, %2" : "=v"(v) : "v"((lane & 7) * 128), "s"(p) : "memory");
  }
  return v;
}
DI unsigned load_gen(const Args& A) {
  unsigned v;
  asm volatile("global_load_dword %0, %1, %2 sc1" : "=v"(v) : "v"(0), "s"(A.ws + F_GEN) : "memory");
  return v;
}
DI unsigned epoch_of(unsigned genv) { return __builtin_amdgcn_readfirstlane(genv) * 1664525u + 1013904223u; }

DI int krot(int rb, int cgp) { return cgp < 14 ? (cgp + 2 * rb + (rb >> 2)) % 7 : 0; }
DI int kch(int s, int rot) { const int c = s + rot; return c >= 7 ? c - 7 : c; }

DI constexpr int posA(int s) { return s == 0 || s == 6 ? 139264 : 16384 + (s - 1) * 24576; }
DI unsigned phase1_gu(const Args& A, rsrc_t rs, char* lds, int rb, int cgp, unsigned& tvw, KV& kv STAMP_ARG) {
  const int tid = threadIdx.x, lane = tid & 63, wid = __builtin_amdgcn_readfirstlane(tid >> 6);
  const int hh = lane >> 5, l31 = lane & 31, rsb = wid & 3, cg = wid >> 2;
  const int q4 = (lane & 15) >> 2, p4 = lane & 3, blk = (lane >> 4) & 1;
  const unsigned lds0 = (unsigned)(uintptr_t)lds;
  const lds_cptr L = (lds_cptr)(LAS char*)(uintptr_t)lds0;
  const int r8 = lane >> 3, c8 = lane & 7;
  const int xr0 = 16 * wid + r8;
  const char* xs = A.x + (size_t)(128 * rb + 16 * wid) * XROW;
  const unsigned xc0 = r8 * XROW + 16 * (c8 ^ ((xr0 >> 1) & 7)), xc1 = (r8 + 8) * XROW + 16 * (c8 ^ (((xr0 + 8) >> 1) & 7));
  const int sw0 = 32 * ((c8 >> 1) ^ (r8 & 3)) + 16 * (c8 & 1);
  const unsigned oq = r8 * QROW + sw0, og = r8 * FROW + sw0, ou = og ^ 32;
  const char* wq = A.wqkv + (size_t)(8 * wid) * QROW + 128 * cgp;
  const char* wg = A.wg + (size_t)(8 * wid) * FROW + 256 * cgp;
  const char* wu = A.wu + (size_t)(8 * wid) * FROW + 256 * cgp;
  const int rot = krot(rb, cgp);
  auto pieceA = [&](int s, int p) {
    const unsigned sb = lds0 + posA(s);
    const int c = kch(s, rot);
    if (p == 0) glds16(xs + 128 * c, xc0, sb + 2048 * wid);
    if (p == 1) glds16(xs + 128 * c, xc1, sb + 2048 * wid + 1024);
    if (p == 2) glds16(wq + c * (64 * QROW), oq, sb + 16384 + 1024 * wid);
  };
  auto stageA = [&](int s) { pieceA(s, 0); pieceA(s, 1); pieceA(s, 2); };
  auto pieceU = [&](int u, int c, int p) {
    glds16((p ? wu : wg) + kch(c, rot) * (64 * FROW) + 128 * u, p ? ou : og, lds0 + c * 16384 + wid * 2048 + p * 1024);
  };
  STAMP_F(A.ws, 32);
  pieceA(0, 2);
  unsigned genv;
  stageA(1); pieceU(0, 0, 0); pieceU(0, 0, 1); stageA(2);
  STAMP_F(A.ws, 35);

  const int rt32 = rb * 4 + rsb;
  const int xrow = 32 * rsb + l31, xsw = (xrow >> 1) & 7;
  lds_cptr xb[4];
#pragma unroll
  for (int j = 0; j < 4; ++j) xb[j] = L + xrow * 128 + 16 * (((j << 1) | hh) ^ xsw);
  const lds_cptr wt0 = L + 16384 + (8 * hh + q4) * 128 + 8 * p4 + 32 * ((2 * cg + blk) ^ q4);
  const int rowb = hh * 2048 + blk * 1024 + q4 * 128 + 8 * p4;
  const lds_cptr wb0 = L + rowb + 32 * ((2 * cg) ^ q4 ^ blk), wb1 = L + rowb + 32 * ((2 * cg + 1) ^ q4 ^ blk);
  const bool isq = cgp < 7;
  const unsigned hs = isq ? 2048u : 4096u, pad = isq ? 0u : 0x3f80u;
  unsigned qo[2]; bool str[2];
#pragma unroll
  for (int t = 0; t < 2; ++t) {
    const int P = 8 * (isq ? cgp : cgp - 7) + 4 * cg + 2 * t + hh;
    const int hp = (P * 37) >> 8, r = P - 7 * hp;
    const int head = 2 * hp + (r > 3 ? 1 : 0), piece = r > 3 ? r - 4 : r;
    qo[t] = (isq ? WS_Q : WS_KV) + (head * 64 + rt32) * hs + l31 * 16 + piece * 512;
    str[t] = r == 3;
    PIN(qo[t]);
  }
  const float qs = isq ? QSCALE : 1.f;
  unsigned hbase = WS_H + (rt32 * 112 + 8 * cgp + 2 * cg) * 1024 + lane * 16;
  PIN(hbase);
  kv = kv_setup(A.ws, lds0, wid);

  bf16x8 xf[28];
  f32x16 a0, a1, a2, b0, b1, zero16;
#pragma unroll
  for (int i = 0; i < 16; ++i) zero16[i] = 0.f;
  bf16x8 wf[4];
  lds_cptr c0, c1;
  auto rdG = [&](auto Q) {
    constexpr int q = decltype(Q)::v;
    if constexpr (q < 56) {
      constexpr int i = q >> 1;
      if constexpr (q % 28 == 0) { c0 = wb0 + i * 4096; c1 = wb1 + i * 4096; PIN(c0); PIN(c1); }
      const lds_cptr b = (q & 1) ? c1 : c0;
      wf[(q + 2) & 3] = cat8(trread(b + (i % 14) * 4096), trread(b + (i % 14) * 4096 + 512));
    } else if constexpr (q < 112) {
      constexpr int kk = (q - 56) % 28;
      if constexpr (kk % 14 == 0) { c0 = (q < 84 ? wb0 : wb1) + kk * 4096; PIN(c0); }
      wf[(q + 2) & 3] = cat8(trread(c0 + (kk % 14) * 4096), trread(c0 + (kk % 14) * 4096 + 512));
    }
  };

  STAMP_F(A.ws, 36);
  {
    bf16x8 fq[2][4];
    sfor<0, 8>([&](auto S) {
      constexpr int s = decltype(S)::v;
      if constexpr (s < 7) {
        waitv_bar<(s == 1 ? 9 : s < 3 ? 8 : s + 6)>();
        if constexpr (s == 0) STAMP_AT(A.ws, 1); else STAMP_F(A.ws, 11 + 2 * s);
        lds_cptr cq = wt0 + posA(s);
        PIN(cq);
#pragma unroll
        for (int j = 0; j < 4; ++j) {
          xf[4 * s + j] = ldsread16(xb[j] + posA(s));
          fq[s & 1][j] = cat8(trread(cq + j * 2048), trread(cq + j * 2048 + 512));
        }
      } else { rdG(IC<0>{}); rdG(IC<1>{}); rdG(IC<2>{}); }
      SBAR();
      sfor<0, 4>([&](auto J) {
        constexpr int j = decltype(J)::v, i = 4 * (s - 1) + j, n = 4 * s + j;
        if constexpr (s > 0) a0 = MFMA32(fq[(s - 1) & 1][j], xf[i], i == 0 ? zero16 : a0);
        SBAR();
        if constexpr (n < 12) pieceA(3 + n / 3, n % 3);
        else if constexpr (n == 12) genv = load_gen(A);
        else if constexpr (n < 25) pieceU(0, 1 + (n - 13) / 2, (n - 13) & 1);
        SBAR();
      });
    });
  }
  STAMP_AT(A.ws, 2);
  unsigned epoch;
  u32x4 qk[2];
  float hv[8];
  auto poll = [&]() { glds16_dev(A.ws + F_QKV + 1024 * (blockIdx.x & (NREP - 1)), lane * 16, lds0 + VRING + wid * 2048); };
  sfor<0, 112>([&](auto Q) {
    constexpr int q = decltype(Q)::v, i = q - 56;
    if constexpr (q < 64 && (q & 7) == 5) {
      constexpr int c = q >> 3;
      waitv_bar_nl<(c == 0 ? 12 : c == 1 ? 10 : c == 2 ? 10 : c == 3 ? 4 : c == 6 ? 6 : c == 7 ? 4 : 20)>();
      if constexpr (c == 0) { PIN(genv); epoch = epoch_of(genv); }
      if constexpr (c == 3) { STAMP_F(A.ws, 27); set_qkv_flag(rs, epoch); }
      if constexpr (c == 6) STAMP_F(A.ws, 29);
    }
    if constexpr (i == 13) { waitv_bar_nl<2>(); STAMP_F(A.ws, 30); }
    if constexpr (i == 21) { waitv_bar_nl<1>(); STAMP_F(A.ws, 31); }
    if constexpr (i == QKV_STEP) STAMP_F(A.ws, 37);
    if constexpr (i == QKV_STEP) {
      if (wid == 0) {
        asm volatile("s_waitcnt vmcnt(0)" ::: "memory");
        const u32x4 f = __builtin_bit_cast(u32x4, ldsread16(L + VRING + lane * 16));
        if (!__all(lane >= 56 || (f[0] == epoch && f[1] == epoch && f[2] == epoch && f[3] == epoch))) wait_flags256(rs, F_QKV + 1024 * (blockIdx.x & (NREP - 1)), epoch, lane);
      }
      waitv_bar_nl<20>();
      STAMP_F(A.ws, 38);
    }
    rdG(IC<q + 3>{});
    SBAR();
    if constexpr (q < 56) {
      if constexpr (q & 1) a2 = MFMA32(wf[(q + 2) & 3], xf[q >> 1], q < 2 ? zero16 : a2);
      else a1 = MFMA32(wf[(q + 2) & 3], xf[q >> 1], q < 2 ? zero16 : a1);
    } else if constexpr (i < 28) b0 = MFMA32(wf[(q + 2) & 3], xf[i], i == 0 ? zero16 : b0);
    else b1 = MFMA32(wf[(q + 2) & 3], xf[i - 28], i == 28 ? zero16 : b1);
    SBAR();
    if constexpr (q >= 13 && q < 64 && ((q & 7) == 5 || (q & 7) == 6)) pieceU(1, (q >> 3) - 1, (q & 7) - 5);
    if constexpr (q == 0 || q == 2) {
      constexpr int t = q >> 1;
      const unsigned w0 = pk2(a0[8 * t] * qs, a0[8 * t + 1] * qs), w1 = pk2(a0[8 * t + 2] * qs, a0[8 * t + 3] * qs);
      const unsigned w2 = pk2(a0[8 * t + 4] * qs, a0[8 * t + 5] * qs), w3 = pk2(a0[8 * t + 6] * qs, a0[8 * t + 7] * qs);
      auto r0 = __builtin_amdgcn_permlane32_swap(w0, w2, false, false);
      auto r1 = __builtin_amdgcn_permlane32_swap(w1, w3, false, false);
      qk[t] = u32x4{r0[0], r1[0], r0[1], r1[1]};
      PIN(qk[t]);
    }
    if constexpr (q == 1 || q == 3) {
      constexpr int t = q >> 1;
      u32x4 v = qk[t];
      if (str[t]) { v[2] = pad; v[3] = 0u; }
      st16(rs, qo[t], v);
      if (str[t]) st16(rs, qo[t] + 64 * hs, u32x4{qk[t][2], qk[t][3], pad, 0u});
    }
    if constexpr (i >= 1 && i < 52 && (i - 1) % 17 < 16) {
      constexpr int t = (i - 1) / 17, e = ((i - 1) % 17) >> 1;
      const f32x16& a = t == 0 ? a1 : t == 1 ? a2 : b0;
      if constexpr (((i - 1) % 17) & 1) hv[e] = a[e + 8] * __builtin_amdgcn_rcpf(1.f + hv[e]);
      else hv[e] = EX2(-LOG2E * a[e]);
      PIN(hv[e]);
    }
    if constexpr (i == 17) st16(rs, hbase, pack8(hv));
    if constexpr (i == 34) st16(rs, hbase + 1024, pack8(hv));
    if constexpr (i == 51) st16(rs, hbase + 4096, pack8(hv));
    if constexpr (i == 35) tvw = touch(A, wid, lane, 4);
    if constexpr (i == 0) { if (wid == 0) poll(); }
    if constexpr (i >= QKV_STEP && i < QKV_STEP + 4) p2_first(kv, lds0, wid, lane * 16, i - QKV_STEP);
    SBAR();
  });
#pragma unroll
  for (int i = 0; i < 8; ++i) hv[i] = sigm_mul(b1[i], b1[i + 8]);
  st16(rs, hbase + 5120, pack8(hv));
  STAMP_AT(A.ws, 3);
  return epoch;
}

DI unsigned phase1_v(const Args& A, rsrc_t rs, char* lds, int rb, int cgp, unsigned& tvw, KV& kv STAMP_ARG) {
  const int tid = threadIdx.x, lane = tid & 63, wid = __builtin_amdgcn_readfirstlane(tid >> 6);
  const int hh = lane >> 5, l31 = lane & 31, rsb = wid & 3, cg = wid >> 2;
  const int q4 = (lane & 15) >> 2, p4 = lane & 3, blk = (lane >> 4) & 1;
  const unsigned lds0 = (unsigned)(uintptr_t)lds;
  const lds_cptr L = (lds_cptr)(LAS char*)(uintptr_t)lds0;
  const int r8 = lane >> 3, c8 = lane & 7;
  const int xr0 = 16 * wid + r8;
  const char* xs = A.x + (size_t)(128 * rb + 16 * wid) * XROW;
  const unsigned xc0 = r8 * XROW + 16 * (c8 ^ ((xr0 >> 1) & 7)), xc1 = (r8 + 8) * XROW + 16 * (c8 ^ (((xr0 + 8) >> 1) & 7));
  const unsigned oq = r8 * QROW + 32 * ((c8 >> 1) ^ (r8 & 3)) + 16 * (c8 & 1);
  const int l0 = cgp == 14 ? 14 : 18;
  const char* wa = A.wqkv + (size_t)(8 * wid) * QROW + 128 * l0;
  const char* wbs = A.wqkv + 128 * 17 + 64 * (cgp - 14);
  auto piece = [&](int s, int p) {
    if (s < 7) {
      const unsigned sb = lds0 + pos1(s);
      if (p == 0) glds16(xs + 128 * s, xc0, sb + 2048 * wid);
      if (p == 1) glds16(xs + 128 * s, xc1, sb + 2048 * wid + 1024);
      if (p >= 2) glds16(wa + (size_t)(64 * s) * QROW + 128 * (p - 2), oq, sb + 16384 + 8192 * (p - 2) + 1024 * wid);
    }
  };
  auto piece17 = [&](int p) {
    const int g = wid < 7 ? 4 * wid + p : 24 + p;
    glds16(wbs + (size_t)(16 * g) * QROW, (lane >> 2) * QROW + (lane & 3) * 16, lds0 + 122880 + g * 1024);
  };
#pragma unroll
  for (int p = 2; p < 5; ++p) piece(0, p);
  unsigned tv0 = touch(A, wid, lane, 0), genv;
#pragma unroll
  for (int p = 0; p < 5; ++p) piece(1, p);
  const int rt32 = rb * 4 + rsb;
  const int xrow = 32 * rsb + l31, xsw = (xrow >> 1) & 7;
  lds_cptr xb[4];
#pragma unroll
  for (int j = 0; j < 4; ++j) xb[j] = L + xrow * 128 + 16 * (((j << 1) | hh) ^ xsw);
  const lds_cptr wt0 = L + 16384 + (8 * hh + q4) * 128 + 8 * p4 + 32 * ((2 * cg + blk) ^ q4);
  lds_cptr wb0 = L + 122880 + (8 * hh + q4) * 64 + 32 * blk + 8 * p4;
  PIN(wb0);
  unsigned vo[4];
#pragma unroll
  for (int t = 0; t < 4; ++t) {
    const int tile = t < 3 ? 2 * l0 + 2 * t + cg : 34 + cgp - 14;
    const int n = 32 * (tile - 28) + l31, head = (n * 2341) >> 16, dh = n - 28 * head;
    vo[t] = WS_KV + ((head * 64 + rt32) * 4 + 2) * 1024 + (dh + 32 * hh) * 16;
    PIN(vo[t]);
  }
  bf16x8 xf[28];
  f32x16 a0, a1, a2, b0, zero16;
#pragma unroll
  for (int i = 0; i < 16; ++i) zero16[i] = 0.f;
  bf16x8 wf[6];
  lds_cptr cx[4], c0;
  auto rdA = [&](auto I, auto M) {
    constexpr int i = decltype(I)::v, m = decltype(M)::v, s = i >> 2, j = i & 3, w = (i & 1) * 3;
    if constexpr (m == 0 && j == 0) {
#pragma unroll
      for (int t = 0; t < 4; ++t) { cx[t] = xb[t] + pos1(s); PIN(cx[t]); }
      c0 = wt0 + pos1(s); PIN(c0);
    }
    if constexpr (m == 0) xf[i] = ldsread16(cx[j]);
    wf[w + m] = cat8(trread(c0 + m * 8192 + j * 2048), trread(c0 + m * 8192 + j * 2048 + 512));
  };
  auto rdB = [&](auto I) {
    constexpr int i = decltype(I)::v;
    wf[i & 3] = cat8(trread(wb0 + i * 1024), trread(wb0 + i * 1024 + 256));
  };
  waitv_bar<6>();
  STAMP_AT(A.ws, 1);
  rdA(IC<0>{}, IC<0>{}); rdA(IC<0>{}, IC<1>{}); rdA(IC<0>{}, IC<2>{});
  sfor<0, 28>([&](auto I) {
    constexpr int i = decltype(I)::v, s = i >> 2, w = (i & 1) * 3;
    auto vm = [&](auto M) {
      constexpr int m = decltype(M)::v, r = i & 3;
      if constexpr (m == 0) piece(s + 2, r == 0 ? 0 : r + 1);
      if constexpr (m == 1 && r == 0) piece(s + 2, 1);
      if constexpr (i == 11 && m == 1) genv = load_gen(A);
      if constexpr (i >= 20 && i < 22 && m < 2) piece17(2 * (i - 20) + m);
      if constexpr (i == 22 && m == 0) glds16(A.gw, (lane < 56 ? lane : 55) * 16, lds0 + 151552 + 1024 * wid);
    };
    auto rd = [&](auto M) { if constexpr (i < 27) rdA(IC<i + 1>{}, M); else rdB(M); };
    SBAR();
    a0 = MFMA32(xf[i], wf[w], i == 0 ? zero16 : a0);
    if constexpr ((i & 3) == 3) {
      waitv_bar<(s == 3 ? 5 : s == 5 ? 5 : s == 6 ? 0 : 4)>();
      STAMP_F(A.ws, 13 + 2 * s);
      if constexpr (s == 0) PIN(tv0);
      if constexpr (s == 4) PIN(genv);
    }
    SBAR(); rd(IC<0>{}); vm(IC<0>{}); SBAR();
    a1 = MFMA32(xf[i], wf[w + 1], i == 0 ? zero16 : a1);
    SBAR(); rd(IC<1>{}); vm(IC<1>{}); SBAR();
    a2 = MFMA32(xf[i], wf[w + 2], i == 0 ? zero16 : a2);
    SBAR(); rd(IC<2>{}); vm(IC<2>{}); SBAR();
  });
  STAMP_AT(A.ws, 2);
  const unsigned epoch = epoch_of(genv);
  auto vstore = [&](const f32x16& a, unsigned o) {
    float v[16];
#pragma unroll
    for (int i = 0; i < 16; ++i) v[i] = a[i];
    st16(rs, o, pack8(v)); st16(rs, o + 1024, pack8(v + 8));
  };
  {
    const int head = 8 * (cgp - 14) + 4 * cg + (lane >> 4);
    const unsigned o = WS_KV + ((head * 64 + rt32) * 4 + 2 + ((lane >> 3) & 1)) * 1024 + (28 + (lane & 3) + 32 * ((lane >> 2) & 1)) * 16;
    const unsigned v = (lane & 3) == 0 ? 0x3f803f80u : 0u;
    st16(rs, o, u32x4{v, v, v, v});
  }
  if (cg == 0) {
    sfor<0, 28>([&](auto I) {
      constexpr int i = decltype(I)::v;
      if constexpr (i + 3 < 28) rdB(IC<i + 3>{});
      SBAR();
      b0 = MFMA32(xf[i], wf[i & 3], i == 0 ? zero16 : b0);
      if constexpr (i == 2) vstore(a0, vo[0]);
      if constexpr (i == 5) vstore(a1, vo[1]);
      if constexpr (i == 9) vstore(a2, vo[2]);
      SBAR();
    });
    vstore(b0, vo[3]);
    STAMP_F(A.ws, 27);
    waitv_bar<0>();
    STAMP_F(A.ws, 29);
    set_qkv_flag(rs, epoch);
  } else {
    vstore(a0, vo[0]); vstore(a1, vo[1]); vstore(a2, vo[2]);
    waitv_bar<0>();
    if (cgp == 14) {
      const lds_cptr gp = L + 151552 + 1024 * wid + 16 * hh;
      f32x16 ga = zero16;
#pragma unroll
      for (int i = 0; i < 28; ++i) ga = MFMA32(ldsread16(gp + 32 * i), xf[i], ga);
      const float g = rbf(1.f / (1.f + __expf(-rbf(ga[0]))));
      if (lane < 32) st4(rs, WS_G + 4 * (32 * rt32 + lane), __float_as_uint(g));
    }
  }
  tvw = touch(A, wid, lane, 4);
  kv = kv_setup(A.ws, lds0, wid);
  STAMP_AT(A.ws, 3);
  if (wid == 0) wait_flags256(rs, F_QKV + 1024 * (blockIdx.x & (NREP - 1)), epoch, lane);
  STAMP_F(A.ws, 38);
  __syncthreads();
#pragma unroll
  for (int j = 0; j < 4; ++j) p2_first(kv, lds0, wid, lane * 16, j);
  return epoch;
}

DI float xhalf_max(float v) {
  auto r = __builtin_amdgcn_permlane32_swap(__float_as_uint(v), __float_as_uint(v), false, false);
  return fmaxf(__uint_as_float(r[0]), __uint_as_float(r[1]));
}
DI float max16(const f32x16& s) {
  float a = fmaxf(fmaxf(s[0], s[1]), s[2]), b = fmaxf(fmaxf(s[3], s[4]), s[5]);
  a = fmaxf(fmaxf(a, s[6]), s[7]); b = fmaxf(fmaxf(b, s[8]), s[9]);
  a = fmaxf(fmaxf(a, s[10]), s[11]); b = fmaxf(fmaxf(b, s[12]), s[13]);
  a = fmaxf(fmaxf(a, s[14]), s[15]);
  return fmaxf(a, b);
}

DI void phase23(const Args& A, rsrc_t rs, char* lds, int rb1, int cgp, unsigned epoch, unsigned tvw, const KV& kv STAMP_ARG) {
  const int bid = blockIdx.x, xcd = bid & 7, idx = bid >> 3;
  const int hd = 2 * xcd + (idx & 1), rb = idx >> 1;
  const int i3 = idx < 28 ? idx : idx - 28, rt64 = 4 * xcd + i3 / 7, c7 = i3 % 7;
  const int tid = threadIdx.x, lane = tid & 63, wid = __builtin_amdgcn_readfirstlane(tid >> 6);
  const int hh = lane >> 5, qs = wid & 3, kh = wid >> 2;
  const unsigned lds0 = (unsigned)(uintptr_t)lds;
  const lds_cptr L = (lds_cptr)(LAS char*)(uintptr_t)lds0;
  const unsigned kvo = WS_KV + hd * (64 * 4096) + kh * (32 * 4096);
  const unsigned l16 = lane * 16;
  const int r3 = wid & 1, kq = wid >> 1;
  const int r8 = lane >> 3, c8 = lane & 7;
  const unsigned ow = r8 * XROW + 32 * ((c8 >> 1) ^ (r8 & 3)) + 16 * (c8 & 1);
  const char* wds = A.wd + (size_t)(448 * kq + 16 * r3) * XROW + 128 * c7;
  auto stage_wd = [&](int c, int slot, int p) {
    glds16(wds + (size_t)(32 * c + 8 * p) * XROW, ow, lds0 + slot * 16384 + wid * 2048 + p * 1024);
  };

  waitv_bar<2>();
  STAMP_AT(A.ws, 4);
  const unsigned fvo = F_QKV + 1024 * (bid & (NREP - 1)) + 896;
  unsigned fv = look16(A.ws + fvo, wid, lane, 31);
  const lds_cptr pb = L + VRING + l16;
  const bf16x8 q0 = ldsread16(pb + 8192 + qs * 2048), q1 = ldsread16(pb + 8192 + qs * 2048 + 1024);
  const bf16x8 ka0 = ldsread16(pb + kh * 4096), ka1 = ldsread16(pb + kh * 4096 + 1024);
  bf16x8 kf0 = ldsread16(pb + kh * 4096 + 2048), kf1 = ldsread16(pb + kh * 4096 + 3072);
  f32x16 zero16;
#pragma unroll
  for (int i = 0; i < 16; ++i) zero16[i] = 0.f;
  f32x16 sA = MFMA32(ka0, q0, zero16);
  float mref;
  {
    f32x16 s = MFMA32(ka1, q1, sA);
    mref = rbf(xhalf_max(max16(s)));
  }
  bf16x8 q1m = q1;
  if (hh) q1m[4] = (short)((__float_as_uint(-mref)) >> 16);
  sA = MFMA32(ka1, q1m, sA);
  PIN(kf0); PIN(kf1);
  STAMP_AT(A.ws, 5);
  f32x16 o = zero16, sB;
  bf16x8 vf0, vf1, xf0, xf1;
  u32x4 P0[VLAG + 1], P1[VLAG + 1];
  float e0 = 0.f, e1 = 0.f, e2 = 0.f, e3 = 0.f;
  lds_cptr rk = L + KRING + kh * 4096 + l16, rx = L + VX + kh * 4096 + l16;
  PIN(rk); PIN(rx);
  bf16x8 hf[28];
  unsigned fh;
  float g;
  const unsigned ho = WS_H + ((rt64 * 2 + r3) * 112 + 28 * kq) * 1024 + lane * 16;
#define B8(x) __builtin_bit_cast(bf16x8, x)
#define G(MF, RDS, W0, W1, C, i) do { MF; SBAR(); RDS; SBAR(); \
    float n0 = EX2(C[i]); PIN(n0); SBAR(); W0 = pk2(e0, e1); PIN(W0); SBAR(); float n1 = EX2(C[i + 1]); PIN(n1); SBAR(); W1 = pk2(e2, e3); PIN(W1); SBAR(); \
    float n2 = EX2(C[i + 2]); PIN(n2); float n3 = EX2(C[i + 3]); PIN(n3); e0 = n0; e1 = n1; e2 = n2; e3 = n3; SBAR(); } while (0)
  sfor<0, 16>([&](auto S) {
    constexpr int s = decltype(S)::v;
    constexpr auto D = [](int t) { return t < 0 ? 0 : (t + 2 <= 14 ? 1 : 0) + (t >= 1 && t + 2 <= 15 ? 1 : 0) + (t >= 1 && t <= 3 ? 1 : 0); };
    constexpr auto E = [](int t) { return t < 0 ? 0 : (t < 5 || t == 6 || t == 7) ? 2 : (t >= 8 && t < 15) ? 4 : 0; };
    constexpr int N = s == 0 ? 2 : E(s - 2) + D(s - 1) + E(s - 1);
    if constexpr (s == 1) {
      asm volatile("s_waitcnt vmcnt(3)" : "+v"(fv)::"memory");
      if (wid == 0 && !__all(fv == epoch)) wait_flags(rs, fvo, 32, epoch, lane);
    }
    if constexpr (s == 8) {
      PIN(fh);
      if (wid == 0 && !__all(fh == epoch)) wait_flags(rs, F_H + 128 * (rt64 >> 1), 16, epoch, lane);
    }
    waitv_bar<N>();
    if constexpr (s == 1) { STAMP_F(A.ws, 39); PIN(tvw); }
    if constexpr (s == 1) { if (tid == 0) st4(rs, F_H + 128 * rb1 + 4 * cgp, epoch); }
    if constexpr (s == 5) fh = look16(A.ws + F_H + 128 * (rt64 >> 1), wid, lane);
    auto vm = [&](auto J) {
      constexpr int j = decltype(J)::v, d = D(s), e = j - d;
      if constexpr (j < d) {
        if constexpr (j == 0 && s + 2 <= 14) k_unit(kv, l16, s + 2);
        else if constexpr (j == 2) v_unit(kv, l16, s - 1);
        else v_unit(kv, l16, s + 2);
      } else if constexpr (e < E(s)) {
        if constexpr (s < 8) stage_wd(s < 5 ? s : s - 1, s < 5 ? s : s - 1, e);
        else hf[4 * (s - 8) + e] = ldfrag(rs, ho + (4 * (s - 8) + e) * 1024);
      } else if constexpr (s == 15 && j == 2) g = __uint_as_float(ld4(rs, WS_G + 4 * (rt64 * 64 + r3 * 32 + (lane & 31))));
    };
    SBAR();
    sfor<0, 2>([&](auto T) {
      constexpr int t = decltype(T)::v, i = 2 * s + t;
      constexpr int pc = i < VLAG ? i : VLAG, pp = i - 1 < VLAG ? (i > 0 ? i - 1 : VLAG) : VLAG;
      constexpr bool pv = i >= VLAG, cu = i >= VLAG && i < 2 * VLAG;
      constexpr int ko = (s % 3) * 8192 + t * 2048, vo = 24576 + ko, xo = ((s - 3) % 3) * 8192 + t * 2048;
      f32x16& C = t ? sB : sA;
      f32x16& Nx = t ? sA : sB;
      __builtin_amdgcn_s_setprio(3 - 2 * t);
      G(if constexpr (i < 31) Nx = MFMA32(kf0, q0, zero16),
        if constexpr (cu) { xf0 = ldsread16(rx + xo); xf1 = ldsread16(rx + xo + 1024); } vm(IC<3 * t>{}), P1[pp][2], P1[pp][3], C, 0);
      G(if constexpr (i > VLAG) o = MFMA32(vf1, B8(P1[VLAG]), o),
        if constexpr (pv) vf0 = ldsread16(rk + vo); vm(IC<3 * t + 1>{}), P0[pc][0], P0[pc][1], C, 4);
      if constexpr (cu) { o = MFMA32(xf0, B8(P0[i - VLAG]), o); SBAR(); }
      __builtin_amdgcn_s_setprio(2 - 2 * t);
      G(if constexpr (i < 31) Nx = MFMA32(kf1, q1m, Nx),
        if constexpr (pv) vf1 = ldsread16(rk + vo + 1024); if constexpr (i < 30) { kf0 = ldsread16(rk + ko); kf1 = ldsread16(rk + ko + 1024); }, P0[pc][2], P0[pc][3], C, 8);
      G(if constexpr (pv) o = MFMA32(vf0, B8(P0[VLAG]), o), vm(IC<3 * t + 2>{}), P1[pc][0], P1[pc][1], C, 12);
      if constexpr (cu) { o = MFMA32(xf1, B8(P1[i - VLAG]), o); SBAR(); }
    });
  });
  P1[VLAG][2] = pk2(e0, e1); P1[VLAG][3] = pk2(e2, e3);
  o = MFMA32(vf1, B8(P1[VLAG]), o);
#undef G
  STAMP_AT(A.ws, 6);
  {
    bool bad = false;
#pragma unroll
    for (int i = 0; i < 13; ++i) bad |= !(fabsf(o[i]) <= 1.0e30f);
#pragma unroll
    for (int i = 13; i < 16; ++i) bad |= !hh && !(fabsf(o[i]) <= 1.0e30f);
    if (__any(bad)) {
      float m = -INFINITY;
      o = zero16;
#pragma unroll 1
      for (int t = 0; t < 32; ++t) {
        const unsigned p = kvo + t * 4096 + lane * 16;
        f32x16 s = MFMA32(ldfrag(rs, p), q0, zero16);
        s = MFMA32(ldfrag(rs, p + 1024), q1, s);
        const float mn = fmaxf(m, xhalf_max(max16(s)));
        const float al = EX2(m - mn);
        m = mn;
        float e[16];
#pragma unroll
        for (int i = 0; i < 16; ++i) { o[i] *= al; e[i] = EX2(s[i] - mn); }
        o = MFMA32(ldfrag(rs, p + 2048), __builtin_bit_cast(bf16x8, pack8(e)), o);
        o = MFMA32(ldfrag(rs, p + 3072), __builtin_bit_cast(bf16x8, pack8(e + 8)), o);
      }
      mref = m;
    }
  }
  {
    auto lr = __builtin_amdgcn_permlane32_swap(__float_as_uint(o[12]), __float_as_uint(o[12]), false, false);
    const float lown = __uint_as_float(lr[1]);
    f32x4* xs = (f32x4*)(lds + KRING) + wid * (3 * 64) + lane;
    const f32x4* xr = (const f32x4*)(lds + KRING) + (wid ^ 4) * (3 * 64) + lane;
    f32x4 k0, k1;
    if (kh == 0) { xs[0] = f32x4{o[8], o[9], o[10], o[11]}; xs[64] = f32x4{o[12], o[13], o[14], o[15]}; k0 = f32x4{o[0], o[1], o[2], o[3]}; k1 = f32x4{o[4], o[5], o[6], o[7]}; }
    else { xs[0] = f32x4{o[0], o[1], o[2], o[3]}; xs[64] = f32x4{o[4], o[5], o[6], o[7]}; k0 = f32x4{o[8], o[9], o[10], o[11]}; k1 = f32x4{o[12], o[13], o[14], o[15]}; }
    xs[128] = f32x4{mref, lown, 0.f, 0.f};
    waitv_bar<0>();
    PIN(g);
    const f32x4 t0 = xr[0], t1 = xr[64], ml = xr[128];
    const float M = fmaxf(mref, ml[0]);
    float a0 = EX2(mref - M), a1 = EX2(ml[0] - M);
    const float inv = 1.f / (lown * a0 + ml[1] * a1);
    a0 *= inv; a1 *= inv;
    float v[8];
#pragma unroll
    for (int j = 0; j < 4; ++j) { v[j] = k0[j] * a0 + t0[j] * a1; v[4 + j] = k1[j] * a0 + t1[j] * a1; }
    if (kh && hh) { v[4] = 0.f; v[5] = 0.f; v[6] = 0.f; v[7] = 0.f; }
    st16(rs, WS_O + (((rb * 4 + qs) * 16 + hd) * 2 + kh) * 1024 + l16, pack8(v));
  }
  STAMP_AT(A.ws, 7);
  if (idx >= 28) {
    waitv_bar<0>();
    if (tid == 0) st4(rs, F_O + 128 * rb + 4 * hd, epoch);
    if (bid == 255 && tid == 0) st4(rs, F_GEN, epoch);
    return;
  }

  const int q4 = (lane & 15) >> 2, p4 = lane & 3, blk = (lane >> 4) & 1;
  const int wro = (4 * hh + q4) * 128 + 8 * p4;
  const lds_cptr wd0 = L + kq * 4096 + wro + 32 * ((0 + blk) ^ q4), wd1 = L + kq * 4096 + wro + 32 * ((2 + blk) ^ q4);
  f32x16 bc0, bc1;
  bf16x8 wf[4], of[8];
  lds_cptr d0, d1;
  auto rdW = [&](auto Q) {
    constexpr int q = decltype(Q)::v, sl = (q >> 2) % 10, so = (sl % 3) * 16384 + ((q >> 1) & 1) * 2048;
    if constexpr (q < 56) {
      if constexpr ((q & 3) == 0 && sl % 3 == 0) { d0 = wd0 + sl * 16384; d1 = wd1 + sl * 16384; PIN(d0); PIN(d1); }
      const lds_cptr b = (q & 1) ? d1 : d0;
      wf[q & 3] = cat8(trread(b + so), trread(b + so + 1024));
    }
  };
  const char* wos = A.wo + (size_t)(56 * wid) * XROW + 128 * c7;
  const unsigned oo = WS_O + (((rt64 * 2 + r3) * 16 + 4 * kq) * 2) * 1024 + l16;
  unsigned fo;
  rdW(IC<0>{}); rdW(IC<1>{}); rdW(IC<2>{});
  sfor<0, 56>([&](auto Q) {
    constexpr int q = decltype(Q)::v;
    if constexpr (q == 2) waitv_bar_nl<20>();
    if constexpr (q == 18) {
      waitv_bar_nl<6>();
      if (tid == 0) st4(rs, F_O + 128 * rb + 4 * hd, epoch);
    }
    if constexpr (q == 25) waitv_bar_nl<7>();
    if constexpr (q == 34) waitv_bar_nl<20>();
    if constexpr (q == 37) waitv_bar_nl<4>();
    if constexpr (q == O_STEP) {
      STAMP_AT(A.ws, 8);
      asm volatile("s_waitcnt vmcnt(7)" : "+v"(fo)::"memory");
      if (wid == 0 && !__all(fo == epoch)) wait_flags(rs, F_O + 128 * (rt64 >> 1), 16, epoch, lane);
      waitv_bar_nl<20>();
      STAMP_AT(A.ws, 9);
    }
    rdW(IC<q + 3>{});
    SBAR();
    if constexpr (q & 1) bc1 = MFMA32(wf[q & 3], hf[q >> 1], q < 2 ? zero16 : bc1);
    else bc0 = MFMA32(wf[q & 3], hf[q >> 1], q < 2 ? zero16 : bc0);
    SBAR();
    if constexpr (q >= 2 && q < 8) stage_wd(7 + (q - 2) / 2, 7 + (q - 2) / 2, q & 1);
    if constexpr (q >= 18 && q < 26) stage_wd(10 + (q - 18) / 2, (q - 18) / 2, q & 1);
    if constexpr (q == 33) fo = look16(A.ws + F_O + 128 * (rt64 >> 1), wid, lane);
    if constexpr (q >= 34 && q < 41) glds16(wos + (size_t)(8 * (q - 34)) * XROW, ow, lds0 + 65536 + wid * 7168 + (q - 34) * 1024);
    if constexpr (q >= O_STEP && q < O_STEP + 8) of[q - O_STEP] = ldfrag(rs, oo + (q - O_STEP) * 1024);
    SBAR();
  });
  const float gm = rbf(1.f - g);
#pragma unroll
  for (int i = 0; i < 16; ++i) { bc0[i] *= gm; bc1[i] *= gm; }
  waitv_bar<8>();
  STAMP_AT(A.ws, 10);
  f32x16 ac0 = zero16, ac1 = zero16;
  {
    lds_cptr wo0 = L + 65536 + kq * (112 * 128) + wro + 32 * ((0 + blk) ^ q4), wo1 = L + 65536 + kq * (112 * 128) + wro + 32 * ((2 + blk) ^ q4);
    lds_cptr wo0h = wo0 + (hh ? 2048 : 3072), wo1h = wo1 + (hh ? 2048 : 3072);
    PIN(wo0); PIN(wo1); PIN(wo0h); PIN(wo1h);
#pragma unroll
    for (int h = 0; h < 4; ++h) {
      const int b = h * 28 * 128;
      ac0 = MFMA32(cat8(trread(wo0 + b), trread(wo0 + b + 1024)), of[2 * h], ac0);
      ac1 = MFMA32(cat8(trread(wo1 + b), trread(wo1 + b + 1024)), of[2 * h], ac1);
      ac0 = MFMA32(cat8(trread(wo0 + b + 2048), trread(wo0h + b)), of[2 * h + 1], ac0);
      ac1 = MFMA32(cat8(trread(wo1 + b + 2048), trread(wo1h + b)), of[2 * h + 1], ac1);
    }
  }
  {
    char* rp = lds + (kq < 3 ? kq * (64 * 272) : 122880) + (r3 * 32 + (lane & 31)) * 272 + 16 * hh;
#pragma unroll
    for (int i = 0; i < 4; ++i) {
      *(f32x4*)(rp + 32 * i) = f32x4{g * ac0[4 * i] + bc0[4 * i], g * ac0[4 * i + 1] + bc0[4 * i + 1], g * ac0[4 * i + 2] + bc0[4 * i + 2], g * ac0[4 * i + 3] + bc0[4 * i + 3]};
      *(f32x4*)(rp + 128 + 32 * i) = f32x4{g * ac1[4 * i] + bc1[4 * i], g * ac1[4 * i + 1] + bc1[4 * i + 1], g * ac1[4 * i + 2] + bc1[4 * i + 2], g * ac1[4 * i + 3] + bc1[4 * i + 3]};
    }
  }
  __syncthreads();
  {
    const int row = tid >> 3, cc = tid & 7;
    const char* rp = lds + row * 272 + cc * 32;
    f32x4 s0 = *(const f32x4*)rp, s1 = *(const f32x4*)(rp + 16);
#pragma unroll
    for (int k = 1; k < 4; ++k) { const int ko = k < 3 ? k * (64 * 272) : 122880; s0 += *(const f32x4*)(rp + ko); s1 += *(const f32x4*)(rp + ko + 16); }
    u32x4 w = {pk2(s0[0], s0[1]), pk2(s0[2], s0[3]), pk2(s1[0], s1[1]), pk2(s1[2], s1[3])};
    *(u32x4*)(A.out + (size_t)(rt64 * 64 + row) * XROW + c7 * 128 + cc * 16) = w;
  }
  STAMP_AT(A.ws, 11);
  asm volatile("fused_code_end:");
}

__global__ __launch_bounds__(512, 2) void fused_kernel(Args A) {
  extern __shared__ __attribute__((aligned(16))) char lds[];
  const int bid = blockIdx.x, xcd = bid & 7, idx = bid >> 3;
  const int rb1 = 4 * (xcd & 3) + (idx & 3), cgp = idx < 28 ? 7 * (xcd >> 2) + (idx >> 2) : 14 + (xcd >> 2);
  STAMP_AT(A.ws, 0);
  {
    const int lane = threadIdx.x & 63, wid = __builtin_amdgcn_readfirstlane(threadIdx.x >> 6);
    const int r8 = lane >> 3, c8 = lane & 7, xr0 = 16 * wid + r8;
    const char* xs = A.x + (size_t)(128 * rb1 + 16 * wid) * XROW + 128 * krot(rb1, cgp);
    const unsigned sb = (unsigned)(uintptr_t)lds + (cgp < 14 ? posA(0) : pos1(0)) + 2048 * wid;
    glds16(xs, r8 * XROW + 16 * (c8 ^ ((xr0 >> 1) & 7)), sb);
    glds16(xs, (r8 + 8) * XROW + 16 * (c8 ^ (((xr0 + 8) >> 1) & 7)), sb + 1024);
  }
  unsigned cv = 0;
  {
    unsigned long long pc; unsigned len;
    asm volatile("s_getpc_b64 %0\nfused_code_begin:\n\ts_mov_b32 %1, fused_code_end-fused_code_begin" : "=s"(pc), "=s"(len));
    const unsigned off = (idx * 16 + (threadIdx.x & 15)) * 128;
    if (threadIdx.x < 16 && off < len) asm volatile("global_load_dword %0, %1, %2" : "=v"(cv) : "v"(off), "s"(pc) : "memory");
  }
  const rsrc_t rs = __builtin_amdgcn_make_buffer_rsrc((void*)A.ws, 0, WS_END, 0x00020000);
  unsigned epoch;
  unsigned tvw = 0;
  KV kv;
  if (cgp < 14) epoch = phase1_gu(A, rs, lds, rb1, cgp, tvw, kv STAMP_PASS);
  else epoch = phase1_v(A, rs, lds, rb1, cgp, tvw, kv STAMP_PASS);
  PIN(cv);
  phase23(A, rs, lds, rb1, cgp, epoch, tvw, kv STAMP_PASS);
  STAMP_FLUSH(A.ws);
}

extern "C" void kernel_launch(void* const* d_in, const int* in_sizes, int n_in, void* d_out, int out_size, void* d_ws, size_t ws_size,
                              hipStream_t stream) {
  Args A;
  A.x = (const char*)d_in[0]; A.wqkv = (const char*)d_in[1]; A.wo = (const char*)d_in[2];
  A.wg = (const char*)d_in[4]; A.wu = (const char*)d_in[5]; A.wd = (const char*)d_in[6]; A.gw = (const char*)d_in[7];
  A.ws = (char*)d_ws; A.out = (char*)d_out;
  (void)hipFuncSetAttribute((const void*)fused_kernel, hipFuncAttributeMaxDynamicSharedMemorySize, LDS_BYTES);
  hipLaunchKernelGGL(fused_kernel, dim3(256), dim3(512), LDS_BYTES, stream, A);
}
```

```cpp
#define STAMP
#include <hip/hip_runtime.h>
#include <hip/hip_bf16.h>
#include <stdint.h>

typedef __attribute__((ext_vector_type(8))) short bf16x8;
typedef __attribute__((ext_vector_type(4))) short s16x4;
typedef __attribute__((ext_vector_type(16))) float f32x16;
typedef __attribute__((ext_vector_type(4))) float f32x4;
typedef __attribute__((ext_vector_type(4))) unsigned u32x4;
typedef __attribute__((ext_vector_type(2))) unsigned u32x2;
typedef __attribute__((ext_vector_type(2))) float f32x2;
typedef __attribute__((ext_vector_type(2))) __bf16 bf16x2_t;
#define LAS __attribute__((address_space(3)))
#define GAS __attribute__((address_space(1)))
typedef LAS const char* lds_cptr;
typedef LAS char* lds_ptr;
typedef __amdgpu_buffer_rsrc_t rsrc_t;

#define DI __device__ __forceinline__
#define MFMA32(a, b, c) __builtin_amdgcn_mfma_f32_32x32x16_bf16((a), (b), (c), 0, 0, 0)
#define SBAR() __builtin_amdgcn_sched_barrier(0)
#define PIN(x) asm volatile("" : "+v"(x))
#define EX2(x) __builtin_amdgcn_exp2f(x)

struct Stamps { unsigned t[40]; };
#define STAMP_ARG , Stamps& ST
#define STAMP_PASS , ST
#define STAMP_AT(ws, slot) do { SBAR(); ST.t[slot] = (unsigned)__builtin_amdgcn_s_memrealtime(); SBAR(); } while (0)
#define STAMP_FLUSH(ws) do { if (threadIdx.x == 0) { unsigned* sp = (unsigned*)((char*)(ws) + (64u << 20)) + blockIdx.x * 64; _Pragma("unroll") for (int i_ = 0; i_ < 40; ++i_) sp[i_] = ST.t[i_]; } } while (0)
#define STAMP_F(ws, slot) do {} while (0)

constexpr int D_ = 448, F_ = 1792;
constexpr int XROW = D_ * 2, QROW = 3 * D_ * 2, FROW = F_ * 2;
constexpr float QSCALE = 0.18898223650461362f * 1.4426950408889634f;
constexpr float LOG2E = 1.4426950408889634f;

constexpr unsigned WS_Q = 0;
constexpr unsigned WS_KV = WS_Q + (2u << 20);
constexpr unsigned WS_H = WS_KV + (4u << 20);
constexpr unsigned WS_O = WS_H + (7u << 20);
constexpr unsigned WS_G = WS_O + (2u << 20);
constexpr unsigned WS_F = WS_G + 8192;
constexpr unsigned F_GEN = WS_F, F_H = WS_F + 4096, F_O = WS_F + 8192, F_QKV = WS_F + 16384;
#define NREP 32
constexpr unsigned WS_END = WS_F + 16384 + 65536;

template <int I> struct IC { static constexpr int v = I; };
template <int B, int E, class Fn> DI void sfor(Fn&& f) {
  if constexpr (B < E) { f(IC<B>{}); sfor<B + 1, E>(f); }
}

DI unsigned pk2(float lo, float hi) {
  f32x2 v = {lo, hi};
  bf16x2_t r = __builtin_convertvector(v, bf16x2_t);
  return __builtin_bit_cast(unsigned, r);
}
DI float rbf(float x) { return __uint_as_float(pk2(x, 0.f) << 16); }
DI u32x4 pack8(const float* v) { return u32x4{pk2(v[0], v[1]), pk2(v[2], v[3]), pk2(v[4], v[5]), pk2(v[6], v[7])}; }

DI void glds16(const char* base, unsigned voff, unsigned lds_base) {
  asm volatile("s_mov_b32 m0, %2\n\ts_nop 0\n\tglobal_load_lds_dwordx4 %0, %1" ::"v"(voff), "s"(base), "s"(lds_base) : "memory");
}
DI void glds16_dev(const char* base, unsigned voff, unsigned lds_base) {
  asm volatile("s_mov_b32 m0, %2\n\ts_nop 0\n\tglobal_load_lds_dwordx4 %0, %1 sc1" ::"v"(voff), "s"(base), "s"(lds_base) : "memory");
}
typedef short v4i16_t __attribute__((ext_vector_type(4)));
DI s16x4 trread(lds_cptr p) {
  return __builtin_bit_cast(s16x4, __builtin_amdgcn_ds_read_tr16_b64_v4i16((LAS v4i16_t*)p));
}
DI bf16x8 ldsread16(lds_cptr p) { return *(const LAS bf16x8*)p; }
DI bf16x8 cat8(s16x4 a, s16x4 b) { return __builtin_shufflevector(a, b, 0, 1, 2, 3, 4, 5, 6, 7); }
template <int N> DI void waitv_bar() {
  SBAR();
  asm volatile("s_waitcnt vmcnt(%0) lgkmcnt(0)\n\ts_barrier" ::"i"(N) : "memory");
  SBAR();
}
template <int N> DI void waitv_bar_nl() {
  SBAR();
  asm volatile("s_waitcnt vmcnt(%0)\n\ts_barrier" ::"i"(N) : "memory");
  SBAR();
}
DI void st16(rsrc_t r, unsigned off, u32x4 v) { __builtin_amdgcn_raw_buffer_store_b128(v, r, off, 0, 16); }
DI void st4(rsrc_t r, unsigned off, unsigned v) { __builtin_amdgcn_raw_buffer_store_b32(v, r, off, 0, 16); }
DI u32x4 ld16(rsrc_t r, unsigned off) { return __builtin_amdgcn_raw_buffer_load_b128(r, off, 0, 16); }
DI unsigned ld4(rsrc_t r, unsigned off) { return __builtin_amdgcn_raw_buffer_load_b32(r, off, 0, 16); }
DI bf16x8 ldfrag(rsrc_t r, unsigned off) { return __builtin_bit_cast(bf16x8, ld16(r, off)); }

struct Args {
  const char *x, *wqkv, *wo, *wg, *wu, *wd, *gw;
  char* ws;
  char* out;
};

constexpr int LDS_BYTES = 163840;
constexpr int SLOT = 40960;
DI constexpr int pos1(int s) { return (s % 3) * SLOT; }

#define QSTEP 30
constexpr int QKV_STEP = QSTEP, O_STEP = 44;
constexpr int SPIN_CAP = 100000;
DI void wait_flags(rsrc_t rs, unsigned off, int n, unsigned epoch, int lane) {
  const unsigned o = off + 4 * (lane < n ? lane : 0);
  for (int spin = 0; spin < SPIN_CAP; ++spin) {
    const unsigned v = ld4(rs, o);
    if (__all(v == epoch)) break;
    __builtin_amdgcn_s_sleep(4);
  }
  asm volatile("" ::: "memory");
}
DI void wait_flags256(rsrc_t rs, unsigned off, unsigned epoch, int lane) {
  bool ok = lane >= 56;
  for (int spin = 0; spin < SPIN_CAP; ++spin) {
    const u32x4 v = ld16(rs, off + 16 * (ok ? 55 : lane));
    ok = ok || (v[0] == epoch && v[1] == epoch && v[2] == epoch && v[3] == epoch);
    if (__all(ok)) break;
    __builtin_amdgcn_s_sleep(8);
  }
  asm volatile("" ::: "memory");
}

DI unsigned look16(const char* p, int wid, int lane, int mask = 15) {
  unsigned v = 0;
  asm volatile("s_cmp_eq_u32 %3, 0\n\ts_cselect_b64 exec, -1, 0\n\tglobal_load_dword %0, %1, %2 sc1\n\ts_mov_b64 exec, -1"
               : "+v"(v) : "v"(4 * (lane & mask)), "s"(p), "s"(wid) : "memory", "scc");
  return v;
}
DI float sigm_mul(float g, float u) { return u * __builtin_amdgcn_rcpf(1.f + EX2(-LOG2E * g)); }

constexpr int RING0 = 114688, KRING = RING0, VRING = RING0 + 24576, VX = 90112, VLAG = 6;
struct KV { const char* kvb; const char* pre; unsigned dst; int lt0; };
DI KV kv_setup(const char* ws, unsigned lds0, int wid) {
  const int bid = blockIdx.x, idx = bid >> 3, hd = 2 * (bid & 7) + (idx & 1), rb = idx >> 1;
  KV k;
  k.kvb = ws + WS_KV + hd * (64 * 4096) + (wid >> 2) * (32 * 4096) + (wid & 1) * 1024;
  k.lt0 = ((wid >> 1) & 1) + 2 * rb;
  k.dst = lds0 + wid * 1024;
  k.pre = ws + (wid < 4 ? WS_KV + hd * (64 * 4096) + (wid >> 1) * (32 * 4096) + (((wid & 1) + 2 * rb) & 31) * 4096 : WS_Q + (hd * 64 + rb * 4 + (wid - 4)) * 2048);
  asm volatile("" : "+s"(k.kvb), "+s"(k.pre), "+s"(k.dst), "+s"(k.lt0));
  return k;
}
DI constexpr int vpos(int u) { return u < 3 ? VX + u * 8192 : VRING + (u % 3) * 8192; }
DI void k_unit(const KV& k, unsigned l16, int s) { glds16_dev(k.kvb + (((2 * s + 2 + k.lt0) & 31) << 12), l16, k.dst + KRING + (s % 3) * 8192); }
DI void v_unit(const KV& k, unsigned l16, int u) { glds16_dev(k.kvb + (((2 * u + k.lt0) & 31) << 12) + 2048, l16, k.dst + vpos(u)); }
DI void p2_first(const KV& k, unsigned lds0, int wid, unsigned l16, int j) {
  if (j >= 2) k_unit(k, l16, j - 2);
  else glds16_dev(k.pre + j * 1024, l16, lds0 + VRING + wid * 2048 + j * 1024);
}
DI void set_qkv_flag(rsrc_t rs, unsigned epoch) {
  if (threadIdx.x < NREP) st4(rs, F_QKV + 1024 * threadIdx.x + 4 * blockIdx.x, epoch);
}

DI unsigned touch(const Args& A, int wid, int lane, int r) {
  const int w = blockIdx.x * 8 + wid;
  unsigned v;
  if (r == 0) {
    const int row = (w - 1792) * 2 + ((lane >> 3) & 1), l = lane & 7;
    asm volatile("global_load_dword %0, %1, %2" : "=v"(v) : "v"((row < 448 ? row : 447) * QROW + (14 + (l < 7 ? l : 6)) * 128), "s"(A.wqkv) : "memory");
  } else {
    const char* p = w < 1568 ? A.wd + (size_t)w * 1024 : A.wo + (size_t)(w < 1960 ? w - 1568 : w - 1960) * 1024;
    asm volatile("global_load_dword %0, %1, %2" : "=v"(v) : "v"((lane & 7) * 128), "s"(p) : "memory");
  }
  return v;
}
DI unsigned load_gen(const Args& A) {
  unsigned v;
  asm volatile("global_load_dword %0, %1, %2 sc1" : "=v"(v) : "v"(0), "s"(A.ws + F_GEN) : "memory");
  return v;
}
DI unsigned epoch_of(unsigned genv) { return __builtin_amdgcn_readfirstlane(genv) * 1664525u + 1013904223u; }

DI int krot(int rb, int cgp) { return cgp < 14 ? (cgp + 2 * rb + (rb >> 2)) % 7 : 0; }
DI int kch(int s, int rot) { const int c = s + rot; return c >= 7 ? c - 7 : c; }

DI constexpr int posA(int s) { return s == 0 || s == 6 ? 139264 : 16384 + (s - 1) * 24576; }
DI unsigned phase1_gu(const Args& A, rsrc_t rs, char* lds, int rb, int cgp, unsigned& tvw, KV& kv STAMP_ARG) {
  const int tid = threadIdx.x, lane = tid & 63, wid = __builtin_amdgcn_readfirstlane(tid >> 6);
  const int hh = lane >> 5, l31 = lane & 31, rsb = wid & 3, cg = wid >> 2;
  const int q4 = (lane & 15) >> 2, p4 = lane & 3, blk = (lane >> 4) & 1;
  const unsigned lds0 = (unsigned)(uintptr_t)lds;
  const lds_cptr L = (lds_cptr)(LAS char*)(uintptr_t)lds0;
  const int r8 = lane >> 3, c8 = lane & 7;
  const int xr0 = 16 * wid + r8;
  const char* xs = A.x + (size_t)(128 * rb + 16 * wid) * XROW;
  const unsigned xc0 = r8 * XROW + 16 * (c8 ^ ((xr0 >> 1) & 7)), xc1 = (r8 + 8) * XROW + 16 * (c8 ^ (((xr0 + 8) >> 1) & 7));
  const int sw0 = 32 * ((c8 >> 1) ^ (r8 & 3)) + 16 * (c8 & 1);
  const unsigned oq = r8 * QROW + sw0, og = r8 * FROW + sw0, ou = og ^ 32;
  const char* wq = A.wqkv + (size_t)(8 * wid) * QROW + 128 * cgp;
  const char* wg = A.wg + (size_t)(8 * wid) * FROW + 256 * cgp;
  const char* wu = A.wu + (size_t)(8 * wid) * FROW + 256 * cgp;
  const int rot = krot(rb, cgp);
  auto pieceA = [&](int s, int p) {
    const unsigned sb = lds0 + posA(s);
    const int c = kch(s, rot);
    if (p == 0) glds16(xs + 128 * c, xc0, sb + 2048 * wid);
    if (p == 1) glds16(xs + 128 * c, xc1, sb + 2048 * wid + 1024);
    if (p == 2) glds16(wq + c * (64 * QROW), oq, sb + 16384 + 1024 * wid);
  };
  auto stageA = [&](int s) { pieceA(s, 0); pieceA(s, 1); pieceA(s, 2); };
  auto pieceU = [&](int u, int c, int p) {
    glds16((p ? wu : wg) + kch(c, rot) * (64 * FROW) + 128 * u, p ? ou : og, lds0 + c * 16384 + wid * 2048 + p * 1024);
  };
  STAMP_F(A.ws, 32);
  pieceA(0, 2);
  unsigned genv;
  stageA(1); pieceU(0, 0, 0); pieceU(0, 0, 1); stageA(2);
  STAMP_F(A.ws, 35);

  const int rt32 = rb * 4 + rsb;
  const int xrow = 32 * rsb + l31, xsw = (xrow >> 1) & 7;
  lds_cptr xb[4];
#pragma unroll
  for (int j = 0; j < 4; ++j) xb[j] = L + xrow * 128 + 16 * (((j << 1) | hh) ^ xsw);
  const lds_cptr wt0 = L + 16384 + (8 * hh + q4) * 128 + 8 * p4 + 32 * ((2 * cg + blk) ^ q4);
  const int rowb = hh * 2048 + blk * 1024 + q4 * 128 + 8 * p4;
  const lds_cptr wb0 = L + rowb + 32 * ((2 * cg) ^ q4 ^ blk), wb1 = L + rowb + 32 * ((2 * cg + 1) ^ q4 ^ blk);
  const bool isq = cgp < 7;
  const unsigned hs = isq ? 2048u : 4096u, pad = isq ? 0u : 0x3f80u;
  unsigned qo[2]; bool str[2];
#pragma unroll
  for (int t = 0; t < 2; ++t) {
    const int P = 8 * (isq ? cgp : cgp - 7) + 4 * cg + 2 * t + hh;
    const int hp = (P * 37) >> 8, r = P - 7 * hp;
    const int head = 2 * hp + (r > 3 ? 1 : 0), piece = r > 3 ? r - 4 : r;
    qo[t] = (isq ? WS_Q : WS_KV) + (head * 64 + rt32) * hs + l31 * 16 + piece * 512;
    str[t] = r == 3;
    PIN(qo[t]);
  }
  const float qs = isq ? QSCALE : 1.f;
  unsigned hbase = WS_H + (rt32 * 112 + 8 * cgp + 2 * cg) * 1024 + lane * 16;
  PIN(hbase);
  kv = kv_setup(A.ws, lds0, wid);

  bf16x8 xf[28];
  f32x16 a0, a1, a2, b0, b1, zero16;
#pragma unroll
  for (int i = 0; i < 16; ++i) zero16[i] = 0.f;
  bf16x8 wf[4];
  lds_cptr c0, c1;
  auto rdG = [&](auto Q) {
    constexpr int q = decltype(Q)::v;
    if constexpr (q < 56) {
      constexpr int i = q >> 1;
      if constexpr (q % 28 == 0) { c0 = wb0 + i * 4096; c1 = wb1 + i * 4096; PIN(c0); PIN(c1); }
      const lds_cptr b = (q & 1) ? c1 : c0;
      wf[(q + 2) & 3] = cat8(trread(b + (i % 14) * 4096), trread(b + (i % 14) * 4096 + 512));
    } else if constexpr (q < 112) {
      constexpr int kk = (q - 56) % 28;
      if constexpr (kk % 14 == 0) { c0 = (q < 84 ? wb0 : wb1) + kk * 4096; PIN(c0); }
      wf[(q + 2) & 3] = cat8(trread(c0 + (kk % 14) * 4096), trread(c0 + (kk % 14) * 4096 + 512));
    }
  };

  STAMP_F(A.ws, 36);
  {
    bf16x8 fq[2][4];
    sfor<0, 8>([&](auto S) {
      constexpr int s = decltype(S)::v;
      if constexpr (s < 7) {
        waitv_bar<(s == 1 ? 9 : s < 3 ? 8 : s + 6)>();
        if constexpr (s == 0) STAMP_AT(A.ws, 1); else STAMP_F(A.ws, 11 + 2 * s);
        lds_cptr cq = wt0 + posA(s);
        PIN(cq);
#pragma unroll
        for (int j = 0; j < 4; ++j) {
          xf[4 * s + j] = ldsread16(xb[j] + posA(s));
          fq[s & 1][j] = cat8(trread(cq + j * 2048), trread(cq + j * 2048 + 512));
        }
      } else { rdG(IC<0>{}); rdG(IC<1>{}); rdG(IC<2>{}); }
      SBAR();
      sfor<0, 4>([&](auto J) {
        constexpr int j = decltype(J)::v, i = 4 * (s - 1) + j, n = 4 * s + j;
        if constexpr (s > 0) a0 = MFMA32(fq[(s - 1) & 1][j], xf[i], i == 0 ? zero16 : a0);
        SBAR();
        if constexpr (n < 12) pieceA(3 + n / 3, n % 3);
        else if constexpr (n == 12) genv = load_gen(A);
        else if constexpr (n < 25) pieceU(0, 1 + (n - 13) / 2, (n - 13) & 1);
        SBAR();
      });
    });
  }
  STAMP_AT(A.ws, 2);
  unsigned epoch;
  u32x4 qk[2];
  float hv[8];
  auto poll = [&]() { glds16_dev(A.ws + F_QKV + 1024 * (blockIdx.x & (NREP - 1)), lane * 16, lds0 + VRING + wid * 2048); };
  sfor<0, 112>([&](auto Q) {
    constexpr int q = decltype(Q)::v, i = q - 56;
    if constexpr (q < 64 && (q & 7) == 5) {
      constexpr int c = q >> 3;
      waitv_bar_nl<(c == 0 ? 12 : c == 1 ? 10 : c == 2 ? 10 : c == 3 ? 4 : c == 6 ? 6 : c == 7 ? 4 : 20)>();
      if constexpr (c == 0) { PIN(genv); epoch = epoch_of(genv); }
      if constexpr (c == 3) { STAMP_F(A.ws, 27); set_qkv_flag(rs, epoch); }
      if constexpr (c == 6) STAMP_F(A.ws, 29);
    }
    if constexpr (i == 13) { waitv_bar_nl<2>(); STAMP_F(A.ws, 30); }
    if constexpr (i == 21) { waitv_bar_nl<1>(); STAMP_F(A.ws, 31); }
    if constexpr (i == QKV_STEP) STAMP_F(A.ws, 37);
    if constexpr (i == QKV_STEP) {
      if (wid == 0) {
        asm volatile("s_waitcnt vmcnt(0)" ::: "memory");
        const u32x4 f = __builtin_bit_cast(u32x4, ldsread16(L + VRING + lane * 16));
        if (!__all(lane >= 56 || (f[0] == epoch && f[1] == epoch && f[2] == epoch && f[3] == epoch))) wait_flags256(rs, F_QKV + 1024 * (blockIdx.x & (NREP - 1)), epoch, lane);
      }
      waitv_bar_nl<20>();
      STAMP_F(A.ws, 38);
    }
    rdG(IC<q + 3>{});
    SBAR();
    if constexpr (q < 56) {
      if constexpr (q & 1) a2 = MFMA32(wf[(q + 2) & 3], xf[q >> 1], q < 2 ? zero16 : a2);
      else a1 = MFMA32(wf[(q + 2) & 3], xf[q >> 1], q < 2 ? zero16 : a1);
    } else if constexpr (i < 28) b0 = MFMA32(wf[(q + 2) & 3], xf[i], i == 0 ? zero16 : b0);
    else b1 = MFMA32(wf[(q + 2) & 3], xf[i - 28], i == 28 ? zero16 : b1);
    SBAR();
    if constexpr (q >= 13 && q < 64 && ((q & 7) == 5 || (q & 7) == 6)) pieceU(1, (q >> 3) - 1, (q & 7) - 5);
    if constexpr (q == 0 || q == 2) {
      constexpr int t = q >> 1;
      const unsigned w0 = pk2(a0[8 * t] * qs, a0[8 * t + 1] * qs), w1 = pk2(a0[8 * t + 2] * qs, a0[8 * t + 3] * qs);
      const unsigned w2 = pk2(a0[8 * t + 4] * qs, a0[8 * t + 5] * qs), w3 = pk2(a0[8 * t + 6] * qs, a0[8 * t + 7] * qs);
      auto r0 = __builtin_amdgcn_permlane32_swap(w0, w2, false, false);
      auto r1 = __builtin_amdgcn_permlane32_swap(w1, w3, false, false);
      qk[t] = u32x4{r0[0], r1[0], r0[1], r1[1]};
      PIN(qk[t]);
    }
    if constexpr (q == 1 || q == 3) {
      constexpr int t = q >> 1;
      u32x4 v = qk[t];
      if (str[t]) { v[2] = pad; v[3] = 0u; }
      st16(rs, qo[t], v);
      if (str[t]) st16(rs, qo[t] + 64 * hs, u32x4{qk[t][2], qk[t][3], pad, 0u});
    }
    if constexpr (i >= 1 && i < 52 && (i - 1) % 17 < 16) {
      constexpr int t = (i - 1) / 17, e = ((i - 1) % 17) >> 1;
      const f32x16& a = t == 0 ? a1 : t == 1 ? a2 : b0;
      if constexpr (((i - 1) % 17) & 1) hv[e] = a[e + 8] * __builtin_amdgcn_rcpf(1.f + hv[e]);
      else hv[e] = EX2(-LOG2E * a[e]);
      PIN(hv[e]);
    }
    if constexpr (i == 17) st16(rs, hbase, pack8(hv));
    if constexpr (i == 34) st16(rs, hbase + 1024, pack8(hv));
    if constexpr (i == 51) st16(rs, hbase + 4096, pack8(hv));
    if constexpr (i == 35) tvw = touch(A, wid, lane, 4);
    if constexpr (i == 0) { if (wid == 0) poll(); }
    if constexpr (i >= QKV_STEP && i < QKV_STEP + 4) p2_first(kv, lds0, wid, lane * 16, i - QKV_STEP);
    SBAR();
  });
#pragma unroll
  for (int i = 0; i < 8; ++i) hv[i] = sigm_mul(b1[i], b1[i + 8]);
  st16(rs, hbase + 5120, pack8(hv));
  STAMP_AT(A.ws, 3);
  return epoch;
}

DI unsigned phase1_v(const Args& A, rsrc_t rs, char* lds, int rb, int cgp, unsigned& tvw, KV& kv STAMP_ARG) {
  const int tid = threadIdx.x, lane = tid & 63, wid = __builtin_amdgcn_readfirstlane(tid >> 6);
  const int hh = lane >> 5, l31 = lane & 31, rsb = wid & 3, cg = wid >> 2;
  const int q4 = (lane & 15) >> 2, p4 = lane & 3, blk = (lane >> 4) & 1;
  const unsigned lds0 = (unsigned)(uintptr_t)lds;
  const lds_cptr L = (lds_cptr)(LAS char*)(uintptr_t)lds0;
  const int r8 = lane >> 3, c8 = lane & 7;
  const int xr0 = 16 * wid + r8;
  const char* xs = A.x + (size_t)(128 * rb + 16 * wid) * XROW;
  const unsigned xc0 = r8 * XROW + 16 * (c8 ^ ((xr0 >> 1) & 7)), xc1 = (r8 + 8) * XROW + 16 * (c8 ^ (((xr0 + 8) >> 1) & 7));
  const unsigned oq = r8 * QROW + 32 * ((c8 >> 1) ^ (r8 & 3)) + 16 * (c8 & 1);
  const int l0 = cgp == 14 ? 14 : 18;
  const char* wa = A.wqkv + (size_t)(8 * wid) * QROW + 128 * l0;
  const char* wbs = A.wqkv + 128 * 17 + 64 * (cgp - 14);
  auto piece = [&](int s, int p) {
    if (s < 7) {
      const unsigned sb = lds0 + pos1(s);
      if (p == 0) glds16(xs + 128 * s, xc0, sb + 2048 * wid);
      if (p == 1) glds16(xs + 128 * s, xc1, sb + 2048 * wid + 1024);
      if (p >= 2) glds16(wa + (size_t)(64 * s) * QROW + 128 * (p - 2), oq, sb + 16384 + 8192 * (p - 2) + 1024 * wid);
    }
  };
  auto piece17 = [&](int p) {
    const int g = wid < 7 ? 4 * wid + p : 24 + p;
    glds16(wbs + (size_t)(16 * g) * QROW, (lane >> 2) * QROW + (lane & 3) * 16, lds0 + 122880 + g * 1024);
  };
#pragma unroll
  for (int p = 2; p < 5; ++p) piece(0, p);
  unsigned tv0 = touch(A, wid, lane, 0), genv;
#pragma unroll
  for (int p = 0; p < 5; ++p) piece(1, p);
  const int rt32 = rb * 4 + rsb;
  const int xrow = 32 * rsb + l31, xsw = (xrow >> 1) & 7;
  lds_cptr xb[4];
#pragma unroll
  for (int j = 0; j < 4; ++j) xb[j] = L + xrow * 128 + 16 * (((j << 1) | hh) ^ xsw);
  const lds_cptr wt0 = L + 16384 + (8 * hh + q4) * 128 + 8 * p4 + 32 * ((2 * cg + blk) ^ q4);
  lds_cptr wb0 = L + 122880 + (8 * hh + q4) * 64 + 32 * blk + 8 * p4;
  PIN(wb0);
  unsigned vo[4];
#pragma unroll
  for (int t = 0; t < 4; ++t) {
    const int tile = t < 3 ? 2 * l0 + 2 * t + cg : 34 + cgp - 14;
    const int n = 32 * (tile - 28) + l31, head = (n * 2341) >> 16, dh = n - 28 * head;
    vo[t] = WS_KV + ((head * 64 + rt32) * 4 + 2) * 1024 + (dh + 32 * hh) * 16;
    PIN(vo[t]);
  }
  bf16x8 xf[28];
  f32x16 a0, a1, a2, b0, zero16;
#pragma unroll
  for (int i = 0; i < 16; ++i) zero16[i] = 0.f;
  bf16x8 wf[6];
  lds_cptr cx[4], c0;
  auto rdA = [&](auto I, auto M) {
    constexpr int i = decltype(I)::v, m = decltype(M)::v, s = i >> 2, j = i & 3, w = (i & 1) * 3;
    if constexpr (m == 0 && j == 0) {
#pragma unroll
      for (int t = 0; t < 4; ++t) { cx[t] = xb[t] + pos1(s); PIN(cx[t]); }
      c0 = wt0 + pos1(s); PIN(c0);
    }
    if constexpr (m == 0) xf[i] = ldsread16(cx[j]);
    wf[w + m] = cat8(trread(c0 + m * 8192 + j * 2048), trread(c0 + m * 8192 + j * 2048 + 512));
  };
  auto rdB = [&](auto I) {
    constexpr int i = decltype(I)::v;
    wf[i & 3] = cat8(trread(wb0 + i * 1024), trread(wb0 + i * 1024 + 256));
  };
  waitv_bar<6>();
  STAMP_AT(A.ws, 1);
  rdA(IC<0>{}, IC<0>{}); rdA(IC<0>{}, IC<1>{}); rdA(IC<0>{}, IC<2>{});
  sfor<0, 28>([&](auto I) {
    constexpr int i = decltype(I)::v, s = i >> 2, w = (i & 1) * 3;
    auto vm = [&](auto M) {
      constexpr int m = decltype(M)::v, r = i & 3;
      if constexpr (m == 0) piece(s + 2, r == 0 ? 0 : r + 1);
      if constexpr (m == 1 && r == 0) piece(s + 2, 1);
      if constexpr (i == 11 && m == 1) genv = load_gen(A);
      if constexpr (i >= 20 && i < 22 && m < 2) piece17(2 * (i - 20) + m);
      if constexpr (i == 22 && m == 0) glds16(A.gw, (lane < 56 ? lane : 55) * 16, lds0 + 151552 + 1024 * wid);
    };
    auto rd = [&](auto M) { if constexpr (i < 27) rdA(IC<i + 1>{}, M); else rdB(M); };
    SBAR();
    a0 = MFMA32(xf[i], wf[w], i == 0 ? zero16 : a0);
    if constexpr ((i & 3) == 3) {
      waitv_bar<(s == 3 ? 5 : s == 5 ? 5 : s == 6 ? 0 : 4)>();
      STAMP_F(A.ws, 13 + 2 * s);
      if constexpr (s == 0) PIN(tv0);
      if constexpr (s == 4) PIN(genv);
    }
    SBAR(); rd(IC<0>{}); vm(IC<0>{}); SBAR();
    a1 = MFMA32(xf[i], wf[w + 1], i == 0 ? zero16 : a1);
    SBAR(); rd(IC<1>{}); vm(IC<1>{}); SBAR();
    a2 = MFMA32(xf[i], wf[w + 2], i == 0 ? zero16 : a2);
    SBAR(); rd(IC<2>{}); vm(IC<2>{}); SBAR();
  });
  STAMP_AT(A.ws, 2);
  const unsigned epoch = epoch_of(genv);
  auto vstore = [&](const f32x16& a, unsigned o) {
    float v[16];
#pragma unroll
    for (int i = 0; i < 16; ++i) v[i] = a[i];
    st16(rs, o, pack8(v)); st16(rs, o + 1024, pack8(v + 8));
  };
  {
    const int head = 8 * (cgp - 14) + 4 * cg + (lane >> 4);
    const unsigned o = WS_KV + ((head * 64 + rt32) * 4 + 2 + ((lane >> 3) & 1)) * 1024 + (28 + (lane & 3) + 32 * ((lane >> 2) & 1)) * 16;
    const unsigned v = (lane & 3) == 0 ? 0x3f803f80u : 0u;
    st16(rs, o, u32x4{v, v, v, v});
  }
  if (cg == 0) {
    sfor<0, 28>([&](auto I) {
      constexpr int i = decltype(I)::v;
      if constexpr (i + 3 < 28) rdB(IC<i + 3>{});
      SBAR();
      b0 = MFMA32(xf[i], wf[i & 3], i == 0 ? zero16 : b0);
      if constexpr (i == 2) vstore(a0, vo[0]);
      if constexpr (i == 5) vstore(a1, vo[1]);
      if constexpr (i == 9) vstore(a2, vo[2]);
      SBAR();
    });
    vstore(b0, vo[3]);
    STAMP_F(A.ws, 27);
    waitv_bar<0>();
    STAMP_F(A.ws, 29);
    set_qkv_flag(rs, epoch);
  } else {
    vstore(a0, vo[0]); vstore(a1, vo[1]); vstore(a2, vo[2]);
    waitv_bar<0>();
    if (cgp == 14) {
      const lds_cptr gp = L + 151552 + 1024 * wid + 16 * hh;
      f32x16 ga = zero16;
#pragma unroll
      for (int i = 0; i < 28; ++i) ga = MFMA32(ldsread16(gp + 32 * i), xf[i], ga);
      const float g = rbf(1.f / (1.f + __expf(-rbf(ga[0]))));
      if (lane < 32) st4(rs, WS_G + 4 * (32 * rt32 + lane), __float_as_uint(g));
    }
  }
  tvw = touch(A, wid, lane, 4);
  kv = kv_setup(A.ws, lds0, wid);
  STAMP_AT(A.ws, 3);
  if (wid == 0) wait_flags256(rs, F_QKV + 1024 * (blockIdx.x & (NREP - 1)), epoch, lane);
  STAMP_F(A.ws, 38);
  __syncthreads();
#pragma unroll
  for (int j = 0; j < 4; ++j) p2_first(kv, lds0, wid, lane * 16, j);
  return epoch;
}

DI float xhalf_max(float v) {
  auto r = __builtin_amdgcn_permlane32_swap(__float_as_uint(v), __float_as_uint(v), false, false);
  return fmaxf(__uint_as_float(r[0]), __uint_as_float(r[1]));
}
DI float max16(const f32x16& s) {
  float a = fmaxf(fmaxf(s[0], s[1]), s[2]), b = fmaxf(fmaxf(s[3], s[4]), s[5]);
  a = fmaxf(fmaxf(a, s[6]), s[7]); b = fmaxf(fmaxf(b, s[8]), s[9]);
  a = fmaxf(fmaxf(a, s[10]), s[11]); b = fmaxf(fmaxf(b, s[12]), s[13]);
  a = fmaxf(fmaxf(a, s[14]), s[15]);
  return fmaxf(a, b);
}

DI void phase23(const Args& A, rsrc_t rs, char* lds, int rb1, int cgp, unsigned epoch, unsigned tvw, const KV& kv STAMP_ARG) {
  const int bid = blockIdx.x, xcd = bid & 7, idx = bid >> 3;
  const int hd = 2 * xcd + (idx & 1), rb = idx >> 1;
  const int i3 = idx < 28 ? idx : idx - 28, rt64 = 4 * xcd + i3 / 7, c7 = i3 % 7;
  const int tid = threadIdx.x, lane = tid & 63, wid = __builtin_amdgcn_readfirstlane(tid >> 6);
  const int hh = lane >> 5, qs = wid & 3, kh = wid >> 2;
  const unsigned lds0 = (unsigned)(uintptr_t)lds;
  const lds_cptr L = (lds_cptr)(LAS char*)(uintptr_t)lds0;
  const unsigned kvo = WS_KV + hd * (64 * 4096) + kh * (32 * 4096);
  const unsigned l16 = lane * 16;
  const int r3 = wid & 1, kq = wid >> 1;
  const int r8 = lane >> 3, c8 = lane & 7;
  const unsigned ow = r8 * XROW + 32 * ((c8 >> 1) ^ (r8 & 3)) + 16 * (c8 & 1);
  const char* wds = A.wd + (size_t)(448 * kq + 16 * r3) * XROW + 128 * c7;
  auto stage_wd = [&](int c, int slot, int p) {
    glds16(wds + (size_t)(32 * c + 8 * p) * XROW, ow, lds0 + slot * 16384 + wid * 2048 + p * 1024);
  };

  waitv_bar<2>();
  STAMP_AT(A.ws, 4);
  const unsigned fvo = F_QKV + 1024 * (bid & (NREP - 1)) + 896;
  unsigned fv = look16(A.ws + fvo, wid, lane, 31);
  const lds_cptr pb = L + VRING + l16;
  const bf16x8 q0 = ldsread16(pb + 8192 + qs * 2048), q1 = ldsread16(pb + 8192 + qs * 2048 + 1024);
  const bf16x8 ka0 = ldsread16(pb + kh * 4096), ka1 = ldsread16(pb + kh * 4096 + 1024);
  bf16x8 kf0 = ldsread16(pb + kh * 4096 + 2048), kf1 = ldsread16(pb + kh * 4096 + 3072);
  f32x16 zero16;
#pragma unroll
  for (int i = 0; i < 16; ++i) zero16[i] = 0.f;
  f32x16 sA = MFMA32(ka0, q0, zero16);
  float mref;
  {
    f32x16 s = MFMA32(ka1, q1, sA);
    mref = rbf(xhalf_max(max16(s)));
  }
  bf16x8 q1m = q1;
  if (hh) q1m[4] = (short)((__float_as_uint(-mref)) >> 16);
  sA = MFMA32(ka1, q1m, sA);
  PIN(kf0); PIN(kf1);
  STAMP_AT(A.ws, 5);
  f32x16 o = zero16, sB;
  bf16x8 vf0, vf1, xf0, xf1;
  u32x4 P0[VLAG + 1], P1[VLAG + 1];
  float e0 = 0.f, e1 = 0.f, e2 = 0.f, e3 = 0.f;
  lds_cptr rk = L + KRING + kh * 4096 + l16, rx = L + VX + kh * 4096 + l16;
  PIN(rk); PIN(rx);
  bf16x8 hf[28];
  unsigned fh;
  float g;
  const unsigned ho = WS_H + ((rt64 * 2 + r3) * 112 + 28 * kq) * 1024 + lane * 16;
#define B8(x) __builtin_bit_cast(bf16x8, x)
#define G(MF, RDS, W0, W1, C, i) do { MF; SBAR(); RDS; SBAR(); \
    float n0 = EX2(C[i]); PIN(n0); SBAR(); W0 = pk2(e0, e1); PIN(W0); SBAR(); float n1 = EX2(C[i + 1]); PIN(n1); SBAR(); W1 = pk2(e2, e3); PIN(W1); SBAR(); \
    float n2 = EX2(C[i + 2]); PIN(n2); float n3 = EX2(C[i + 3]); PIN(n3); e0 = n0; e1 = n1; e2 = n2; e3 = n3; SBAR(); } while (0)
  sfor<0, 16>([&](auto S) {
    constexpr int s = decltype(S)::v;
    constexpr auto D = [](int t) { return t < 0 ? 0 : (t + 2 <= 14 ? 1 : 0) + (t >= 1 && t + 2 <= 15 ? 1 : 0) + (t >= 1 && t <= 3 ? 1 : 0); };
    constexpr auto E = [](int t) { return t < 0 ? 0 : (t < 5 || t == 6 || t == 7) ? 2 : (t >= 8 && t < 15) ? 4 : 0; };
    constexpr int N = s == 0 ? 2 : E(s - 2) + D(s - 1) + E(s - 1);
    if constexpr (s == 1) {
      asm volatile("s_waitcnt vmcnt(3)" : "+v"(fv)::"memory");
      if (wid == 0 && !__all(fv == epoch)) wait_flags(rs, fvo, 32, epoch, lane);
    }
    if constexpr (s == 8) {
      PIN(fh);
      if (wid == 0 && !__all(fh == epoch)) wait_flags(rs, F_H + 128 * (rt64 >> 1), 16, epoch, lane);
    }
    waitv_bar<N>();
    if constexpr (s == 1) { STAMP_F(A.ws, 39); PIN(tvw); }
    if constexpr (s == 1) { if (tid == 0) st4(rs, F_H + 128 * rb1 + 4 * cgp, epoch); }
    if constexpr (s == 5) fh = look16(A.ws + F_H + 128 * (rt64 >> 1), wid, lane);
    auto vm = [&](auto J) {
      constexpr int j = decltype(J)::v, d = D(s), e = j - d;
      if constexpr (j < d) {
        if constexpr (j == 0 && s + 2 <= 14) k_unit(kv, l16, s + 2);
        else if constexpr (j == 2) v_unit(kv, l16, s - 1);
        else v_unit(kv, l16, s + 2);
      } else if constexpr (e < E(s)) {
        if constexpr (s < 8) stage_wd(s < 5 ? s : s - 1, s < 5 ? s : s - 1, e);
        else hf[4 * (s - 8) + e] = ldfrag(rs, ho + (4 * (s - 8) + e) * 1024);
      } else if constexpr (s == 15 && j == 2) g = __uint_as_float(ld4(rs, WS_G + 4 * (rt64 * 64 + r3 * 32 + (lane & 31))));
    };
    SBAR();
    sfor<0, 2>([&](auto T) {
      constexpr int t = decltype(T)::v, i = 2 * s + t;
      constexpr int pc = i < VLAG ? i : VLAG, pp = i - 1 < VLAG ? (i > 0 ? i - 1 : VLAG) : VLAG;
      constexpr bool pv = i >= VLAG, cu = i >= VLAG && i < 2 * VLAG;
      constexpr int ko = (s % 3) * 8192 + t * 2048, vo = 24576 + ko, xo = ((s - 3) % 3) * 8192 + t * 2048;
      f32x16& C = t ? sB : sA;
      f32x16& Nx = t ? sA : sB;
      __builtin_amdgcn_s_setprio(3 - 2 * t);
      G(if constexpr (i < 31) Nx = MFMA32(kf0, q0, zero16),
        if constexpr (cu) { xf0 = ldsread16(rx + xo); xf1 = ldsread16(rx + xo + 1024); } vm(IC<3 * t>{}), P1[pp][2], P1[pp][3], C, 0);
      G(if constexpr (i > VLAG) o = MFMA32(vf1, B8(P1[VLAG]), o),
        if constexpr (pv) vf0 = ldsread16(rk + vo); vm(IC<3 * t + 1>{}), P0[pc][0], P0[pc][1], C, 4);
      if constexpr (cu) { o = MFMA32(xf0, B8(P0[i - VLAG]), o); SBAR(); }
      __builtin_amdgcn_s_setprio(2 - 2 * t);
      G(if constexpr (i < 31) Nx = MFMA32(kf1, q1m, Nx),
        if constexpr (pv) vf1 = ldsread16(rk + vo + 1024); if constexpr (i < 30) { kf0 = ldsread16(rk + ko); kf1 = ldsread16(rk + ko + 1024); }, P0[pc][2], P0[pc][3], C, 8);
      G(if constexpr (pv) o = MFMA32(vf0, B8(P0[VLAG]), o), vm(IC<3 * t + 2>{}), P1[pc][0], P1[pc][1], C, 12);
      if constexpr (cu) { o = MFMA32(xf1, B8(P1[i - VLAG]), o); SBAR(); }
    });
  });
  P1[VLAG][2] = pk2(e0, e1); P1[VLAG][3] = pk2(e2, e3);
  o = MFMA32(vf1, B8(P1[VLAG]), o);
#undef G
  STAMP_AT(A.ws, 6);
  {
    bool bad = false;
#pragma unroll
    for (int i = 0; i < 13; ++i) bad |= !(fabsf(o[i]) <= 1.0e30f);
#pragma unroll
    for (int i = 13; i < 16; ++i) bad |= !hh && !(fabsf(o[i]) <= 1.0e30f);
    if (__any(bad)) {
      float m = -INFINITY;
      o = zero16;
#pragma unroll 1
      for (int t = 0; t < 32; ++t) {
        const unsigned p = kvo + t * 4096 + lane * 16;
        f32x16 s = MFMA32(ldfrag(rs, p), q0, zero16);
        s = MFMA32(ldfrag(rs, p + 1024), q1, s);
        const float mn = fmaxf(m, xhalf_max(max16(s)));
        const float al = EX2(m - mn);
        m = mn;
        float e[16];
#pragma unroll
        for (int i = 0; i < 16; ++i) { o[i] *= al; e[i] = EX2(s[i] - mn); }
        o = MFMA32(ldfrag(rs, p + 2048), __builtin_bit_cast(bf16x8, pack8(e)), o);
        o = MFMA32(ldfrag(rs, p + 3072), __builtin_bit_cast(bf16x8, pack8(e + 8)), o);
      }
      mref = m;
    }
  }
  {
    auto lr = __builtin_amdgcn_permlane32_swap(__float_as_uint(o[12]), __float_as_uint(o[12]), false, false);
    const float lown = __uint_as_float(lr[1]);
    f32x4* xs = (f32x4*)(lds + KRING) + wid * (3 * 64) + lane;
    const f32x4* xr = (const f32x4*)(lds + KRING) + (wid ^ 4) * (3 * 64) + lane;
    f32x4 k0, k1;
    if (kh == 0) { xs[0] = f32x4{o[8], o[9], o[10], o[11]}; xs[64] = f32x4{o[12], o[13], o[14], o[15]}; k0 = f32x4{o[0], o[1], o[2], o[3]}; k1 = f32x4{o[4], o[5], o[6], o[7]}; }
    else { xs[0] = f32x4{o[0], o[1], o[2], o[3]}; xs[64] = f32x4{o[4], o[5], o[6], o[7]}; k0 = f32x4{o[8], o[9], o[10], o[11]}; k1 = f32x4{o[12], o[13], o[14], o[15]}; }
    xs[128] = f32x4{mref, lown, 0.f, 0.f};
    waitv_bar<0>();
    PIN(g);
    const f32x4 t0 = xr[0], t1 = xr[64], ml = xr[128];
    const float M = fmaxf(mref, ml[0]);
    float a0 = EX2(mref - M), a1 = EX2(ml[0] - M);
    const float inv = 1.f / (lown * a0 + ml[1] * a1);
    a0 *= inv; a1 *= inv;
    float v[8];
#pragma unroll
    for (int j = 0; j < 4; ++j) { v[j] = k0[j] * a0 + t0[j] * a1; v[4 + j] = k1[j] * a0 + t1[j] * a1; }
    if (kh && hh) { v[4] = 0.f; v[5] = 0.f; v[6] = 0.f; v[7] = 0.f; }
    st16(rs, WS_O + (((rb * 4 + qs) * 16 + hd) * 2 + kh) * 1024 + l16, pack8(v));
  }
  STAMP_AT(A.ws, 7);
  if (idx >= 28) {
    waitv_bar<0>();
    if (tid == 0) st4(rs, F_O + 128 * rb + 4 * hd, epoch);
    if (bid == 255 && tid == 0) st4(rs, F_GEN, epoch);
    return;
  }

  const int q4 = (lane & 15) >> 2, p4 = lane & 3, blk = (lane >> 4) & 1;
  const int wro = (4 * hh + q4) * 128 + 8 * p4;
  const lds_cptr wd0 = L + kq * 4096 + wro + 32 * ((0 + blk) ^ q4), wd1 = L + kq * 4096 + wro + 32 * ((2 + blk) ^ q4);
  f32x16 bc0, bc1;
  bf16x8 wf[4], of[8];
  lds_cptr d0, d1;
  auto rdW = [&](auto Q) {
    constexpr int q = decltype(Q)::v, sl = (q >> 2) % 10, so = (sl % 3) * 16384 + ((q >> 1) & 1) * 2048;
    if constexpr (q < 56) {
      if constexpr ((q & 3) == 0 && sl % 3 == 0) { d0 = wd0 + sl * 16384; d1 = wd1 + sl * 16384; PIN(d0); PIN(d1); }
      const lds_cptr b = (q & 1) ? d1 : d0;
      wf[q & 3] = cat8(trread(b + so), trread(b + so + 1024));
    }
  };
  const char* wos = A.wo + (size_t)(56 * wid) * XROW + 128 * c7;
  const unsigned oo = WS_O + (((rt64 * 2 + r3) * 16 + 4 * kq) * 2) * 1024 + l16;
  unsigned fo;
  rdW(IC<0>{}); rdW(IC<1>{}); rdW(IC<2>{});
  sfor<0, 56>([&](auto Q) {
    constexpr int q = decltype(Q)::v;
    if constexpr (q == 2) waitv_bar_nl<20>();
    if constexpr (q == 18) {
      waitv_bar_nl<6>();
      if (tid == 0) st4(rs, F_O + 128 * rb + 4 * hd, epoch);
    }
    if constexpr (q == 25) waitv_bar_nl<7>();
    if constexpr (q == 34) waitv_bar_nl<20>();
    if constexpr (q == 37) waitv_bar_nl<4>();
    if constexpr (q == O_STEP) {
      STAMP_AT(A.ws, 8);
      asm volatile("s_waitcnt vmcnt(7)" : "+v"(fo)::"memory");
      if (wid == 0 && !__all(fo == epoch)) wait_flags(rs, F_O + 128 * (rt64 >> 1), 16, epoch, lane);
      waitv_bar_nl<20>();
      STAMP_AT(A.ws, 9);
    }
    rdW(IC<q + 3>{});
    SBAR();
    if constexpr (q & 1) bc1 = MFMA32(wf[q & 3], hf[q >> 1], q < 2 ? zero16 : bc1);
    else bc0 = MFMA32(wf[q & 3], hf[q >> 1], q < 2 ? zero16 : bc0);
    SBAR();
    if constexpr (q >= 2 && q < 8) stage_wd(7 + (q - 2) / 2, 7 + (q - 2) / 2, q & 1);
    if constexpr (q >= 18 && q < 26) stage_wd(10 + (q - 18) / 2, (q - 18) / 2, q & 1);
    if constexpr (q == 33) fo = look16(A.ws + F_O + 128 * (rt64 >> 1), wid, lane);
    if constexpr (q >= 34 && q < 41) glds16(wos + (size_t)(8 * (q - 34)) * XROW, ow, lds0 + 65536 + wid * 7168 + (q - 34) * 1024);
    if constexpr (q >= O_STEP && q < O_STEP + 8) of[q - O_STEP] = ldfrag(rs, oo + (q - O_STEP) * 1024);
    SBAR();
  });
  const float gm = rbf(1.f - g);
#pragma unroll
  for (int i = 0; i < 16; ++i) { bc0[i] *= gm; bc1[i] *= gm; }
  waitv_bar<8>();
  STAMP_AT(A.ws, 10);
  f32x16 ac0 = zero16, ac1 = zero16;
  {
    lds_cptr wo0 = L + 65536 + kq * (112 * 128) + wro + 32 * ((0 + blk) ^ q4), wo1 = L + 65536 + kq * (112 * 128) + wro + 32 * ((2 + blk) ^ q4);
    lds_cptr wo0h = wo0 + (hh ? 2048 : 3072), wo1h = wo1 + (hh ? 2048 : 3072);
    PIN(wo0); PIN(wo1); PIN(wo0h); PIN(wo1h);
#pragma unroll
    for (int h = 0; h < 4; ++h) {
      const int b = h * 28 * 128;
      ac0 = MFMA32(cat8(trread(wo0 + b), trread(wo0 + b + 1024)), of[2 * h], ac0);
      ac1 = MFMA32(cat8(trread(wo1 + b), trread(wo1 + b + 1024)), of[2 * h], ac1);
      ac0 = MFMA32(cat8(trread(wo0 + b + 2048), trread(wo0h + b)), of[2 * h + 1], ac0);
      ac1 = MFMA32(cat8(trread(wo1 + b + 2048), trread(wo1h + b)), of[2 * h + 1], ac1);
    }
  }
  {
    char* rp = lds + (kq < 3 ? kq * (64 * 272) : 122880) + (r3 * 32 + (lane & 31)) * 272 + 16 * hh;
#pragma unroll
    for (int i = 0; i < 4; ++i) {
      *(f32x4*)(rp + 32 * i) = f32x4{g * ac0[4 * i] + bc0[4 * i], g * ac0[4 * i + 1] + bc0[4 * i + 1], g * ac0[4 * i + 2] + bc0[4 * i + 2], g * ac0[4 * i + 3] + bc0[4 * i + 3]};
      *(f32x4*)(rp + 128 + 32 * i) = f32x4{g * ac1[4 * i] + bc1[4 * i], g * ac1[4 * i + 1] + bc1[4 * i + 1], g * ac1[4 * i + 2] + bc1[4 * i + 2], g * ac1[4 * i + 3] + bc1[4 * i + 3]};
    }
  }
  __syncthreads();
  {
    const int row = tid >> 3, cc = tid & 7;
    const char* rp = lds + row * 272 + cc * 32;
    f32x4 s0 = *(const f32x4*)rp, s1 = *(const f32x4*)(rp + 16);
#pragma unroll
    for (int k = 1; k < 4; ++k) { const int ko = k < 3 ? k * (64 * 272) : 122880; s0 += *(const f32x4*)(rp + ko); s1 += *(const f32x4*)(rp + ko + 16); }
    u32x4 w = {pk2(s0[0], s0[1]), pk2(s0[2], s0[3]), pk2(s1[0], s1[1]), pk2(s1[2], s1[3])};
    *(u32x4*)(A.out + (size_t)(rt64 * 64 + row) * XROW + c7 * 128 + cc * 16) = w;
  }
  STAMP_AT(A.ws, 11);
  asm volatile("fused_code_end:");
}

__global__ __launch_bounds__(512, 2) void fused_kernel(Args A) {
  extern __shared__ __attribute__((aligned(16))) char lds[];
  const int bid = blockIdx.x, xcd = bid & 7, idx = bid >> 3;
  const int rb1 = 4 * (xcd & 3) + (idx & 3), cgp = idx < 28 ? 7 * (xcd >> 2) + (idx >> 2) : 14 + (xcd >> 2);
  Stamps ST;
#pragma unroll
  for (int i = 0; i < 40; ++i) ST.t[i] = 0;
  STAMP_AT(A.ws, 0);
  {
    const int lane = threadIdx.x & 63, wid = __builtin_amdgcn_readfirstlane(threadIdx.x >> 6);
    const int r8 = lane >> 3, c8 = lane & 7, xr0 = 16 * wid + r8;
    const char* xs = A.x + (size_t)(128 * rb1 + 16 * wid) * XROW + 128 * krot(rb1, cgp);
    const unsigned sb = (unsigned)(uintptr_t)lds + (cgp < 14 ? posA(0) : pos1(0)) + 2048 * wid;
    glds16(xs, r8 * XROW + 16 * (c8 ^ ((xr0 >> 1) & 7)), sb);
    glds16(xs, (r8 + 8) * XROW + 16 * (c8 ^ (((xr0 + 8) >> 1) & 7)), sb + 1024);
  }
  unsigned cv = 0;
  {
    unsigned long long pc; unsigned len;
    asm volatile("s_getpc_b64 %0\nfused_code_begin:\n\ts_mov_b32 %1, fused_code_end-fused_code_begin" : "=s"(pc), "=s"(len));
    const unsigned off = (idx * 16 + (threadIdx.x & 15)) * 128;
    if (threadIdx.x < 16 && off < len) asm volatile("global_load_dword %0, %1, %2" : "=v"(cv) : "v"(off), "s"(pc) : "memory");
  }
  const rsrc_t rs = __builtin_amdgcn_make_buffer_rsrc((void*)A.ws, 0, WS_END, 0x00020000);
  unsigned epoch;
  unsigned tvw = 0;
  KV kv;
  if (cgp < 14) epoch = phase1_gu(A, rs, lds, rb1, cgp, tvw, kv STAMP_PASS);
  else epoch = phase1_v(A, rs, lds, rb1, cgp, tvw, kv STAMP_PASS);
  PIN(cv);
  phase23(A, rs, lds, rb1, cgp, epoch, tvw, kv STAMP_PASS);
  if (blockIdx.x == 8 && threadIdx.x < 64) {
    const unsigned tgt = ST.t[0] + 6000 + 4 * (ST.t[0] - ST.t[0]);
    while ((int)((unsigned)__builtin_amdgcn_s_memrealtime() - tgt) < 0) __builtin_amdgcn_s_sleep(1);
  }
}

extern "C" void kernel_launch(void* const* d_in, const int* in_sizes, int n_in, void* d_out, int out_size, void* d_ws, size_t ws_size,
                              hipStream_t stream) {
  Args A;
  A.x = (const char*)d_in[0]; A.wqkv = (const char*)d_in[1]; A.wo = (const char*)d_in[2];
  A.wg = (const char*)d_in[4]; A.wu = (const char*)d_in[5]; A.wd = (const char*)d_in[6]; A.gw = (const char*)d_in[7];
  A.ws = (char*)d_ws; A.out = (char*)d_out;
  (void)hipFuncSetAttribute((const void*)fused_kernel, hipFuncAttributeMaxDynamicSharedMemorySize, LDS_BYTES);
  hipLaunchKernelGGL(fused_kernel, dim3(256), dim3(512), LDS_BYTES, stream, A);
}
```

```cpp
#include <hip/hip_runtime.h>
#include <hip/hip_bf16.h>
#include <stdint.h>

typedef __attribute__((ext_vector_type(8))) short bf16x8;
typedef __attribute__((ext_vector_type(4))) short s16x4;
typedef __attribute__((ext_vector_type(16))) float f32x16;
typedef __attribute__((ext_vector_type(4))) float f32x4;
typedef __attribute__((ext_vector_type(4))) unsigned u32x4;
typedef __attribute__((ext_vector_type(2))) unsigned u32x2;
typedef __attribute__((ext_vector_type(2))) float f32x2;
typedef __attribute__((ext_vector_type(2))) __bf16 bf16x2_t;
#define LAS __attribute__((address_space(3)))
#define GAS __attribute__((address_space(1)))
typedef LAS const char* lds_cptr;
typedef LAS char* lds_ptr;
typedef __amdgpu_buffer_rsrc_t rsrc_t;

#define DI __device__ __forceinline__
#define MFMA32(a, b, c) __builtin_amdgcn_mfma_f32_32x32x16_bf16((a), (b), (c), 0, 0, 0)
#define SBAR() __builtin_amdgcn_sched_barrier(0)
#define PIN(x) asm volatile("" : "+v"(x))
#define EX2(x) __builtin_amdgcn_exp2f(x)

#define STAMP_F(ws, slot) do {} while (0)
#define STAMP_ARG
#define STAMP_PASS
#define STAMP_AT(ws, slot) do {} while (0)
#define STAMP_FLUSH(ws) do {} while (0)

constexpr int D_ = 448, F_ = 1792;
constexpr int XROW = D_ * 2, QROW = 3 * D_ * 2, FROW = F_ * 2;
constexpr float QSCALE = 0.18898223650461362f * 1.4426950408889634f;
constexpr float LOG2E = 1.4426950408889634f;

constexpr unsigned WS_Q = 0;
constexpr unsigned WS_KV = WS_Q + (2u << 20);
constexpr unsigned WS_H = WS_KV + (4u << 20);
constexpr unsigned WS_O = WS_H + (7u << 20);
constexpr unsigned WS_G = WS_O + (2u << 20);
constexpr unsigned WS_F = WS_G + 8192;
constexpr unsigned F_GEN = WS_F, F_H = WS_F + 4096, F_O = WS_F + 8192, F_QKV = WS_F + 16384;
#define NREP 32
constexpr unsigned WS_END = WS_F + 16384 + 65536;

template <int I> struct IC { static constexpr int v = I; };
template <int B, int E, class Fn> DI void sfor(Fn&& f) {
  if constexpr (B < E) { f(IC<B>{}); sfor<B + 1, E>(f); }
}

DI unsigned pk2(float lo, float hi) {
  f32x2 v = {lo, hi};
  bf16x2_t r = __builtin_convertvector(v, bf16x2_t);
  return __builtin_bit_cast(unsigned, r);
}
DI float rbf(float x) { return __uint_as_float(pk2(x, 0.f) << 16); }
DI u32x4 pack8(const float* v) { return u32x4{pk2(v[0], v[1]), pk2(v[2], v[3]), pk2(v[4], v[5]), pk2(v[6], v[7])}; }

DI void glds16(const char* base, unsigned voff, unsigned lds_base) {
  asm volatile("s_mov_b32 m0, %2\n\ts_nop 0\n\tglobal_load_lds_dwordx4 %0, %1" ::"v"(voff), "s"(base), "s"(lds_base) : "memory");
}
DI void glds16_dev(const char* base, unsigned voff, unsigned lds_base) {
  asm volatile("s_mov_b32 m0, %2\n\ts_nop 0\n\tglobal_load_lds_dwordx4 %0, %1 sc1" ::"v"(voff), "s"(base), "s"(lds_base) : "memory");
}
typedef short v4i16_t __attribute__((ext_vector_type(4)));
DI s16x4 trread(lds_cptr p) {
  return __builtin_bit_cast(s16x4, __builtin_amdgcn_ds_read_tr16_b64_v4i16((LAS v4i16_t*)p));
}
DI bf16x8 ldsread16(lds_cptr p) { return *(const LAS bf16x8*)p; }
DI bf16x8 cat8(s16x4 a, s16x4 b) { return __builtin_shufflevector(a, b, 0, 1, 2, 3, 4, 5, 6, 7); }
template <int N> DI void waitv_bar() {
  SBAR();
  asm volatile("s_waitcnt vmcnt(%0) lgkmcnt(0)\n\ts_barrier" ::"i"(N) : "memory");
  SBAR();
}
template <int N> DI void waitv_bar_nl() {
  SBAR();
  asm volatile("s_waitcnt vmcnt(%0)\n\ts_barrier" ::"i"(N) : "memory");
  SBAR();
}
DI void st16(rsrc_t r, unsigned off, u32x4 v) { __builtin_amdgcn_raw_buffer_store_b128(v, r, off, 0, 16); }
DI void st4(rsrc_t r, unsigned off, unsigned v) { __builtin_amdgcn_raw_buffer_store_b32(v, r, off, 0, 16); }
DI u32x4 ld16(rsrc_t r, unsigned off) { return __builtin_amdgcn_raw_buffer_load_b128(r, off, 0, 16); }
DI unsigned ld4(rsrc_t r, unsigned off) { return __builtin_amdgcn_raw_buffer_load_b32(r, off, 0, 16); }
DI bf16x8 ldfrag(rsrc_t r, unsigned off) { return __builtin_bit_cast(bf16x8, ld16(r, off)); }

struct Args {
  const char *x, *wqkv, *wo, *wg, *wu, *wd, *gw;
  char* ws;
  char* out;
};

constexpr int LDS_BYTES = 163840;
constexpr int SLOT = 40960;
DI constexpr int pos1(int s) { return (s % 3) * SLOT; }

#define QSTEP 30
constexpr int QKV_STEP = QSTEP, O_STEP = 44;
constexpr int SPIN_CAP = 100000;
DI void wait_flags(rsrc_t rs, unsigned off, int n, unsigned epoch, int lane) {
  const unsigned o = off + 4 * (lane < n ? lane : 0);
  for (int spin = 0; spin < SPIN_CAP; ++spin) {
    const unsigned v = ld4(rs, o);
    if (__all(v == epoch)) break;
    __builtin_amdgcn_s_sleep(4);
  }
  asm volatile("" ::: "memory");
}
DI void wait_flags256(rsrc_t rs, unsigned off, unsigned epoch, int lane) {
  bool ok = lane >= 56;
  for (int spin = 0; spin < SPIN_CAP; ++spin) {
    const u32x4 v = ld16(rs, off + 16 * (ok ? 55 : lane));
    ok = ok || (v[0] == epoch && v[1] == epoch && v[2] == epoch && v[3] == epoch);
    if (__all(ok)) break;
    __builtin_amdgcn_s_sleep(8);
  }
  asm volatile("" ::: "memory");
}

DI unsigned look16(const char* p, int wid, int lane, int mask = 15) {
  unsigned v = 0;
  asm volatile("s_cmp_eq_u32 %3, 0\n\ts_cselect_b64 exec, -1, 0\n\tglobal_load_dword %0, %1, %2 sc1\n\ts_mov_b64 exec, -1"
               : "+v"(v) : "v"(4 * (lane & mask)), "s"(p), "s"(wid) : "memory", "scc");
  return v;
}
DI float sigm_mul(float g, float u) { return u * __builtin_amdgcn_rcpf(1.f + EX2(-LOG2E * g)); }

constexpr int RING0 = 114688, KRING = RING0, VRING = RING0 + 24576, VX = 90112, VLAG = 6;
struct KV { const char* kvb; const char* pre; unsigned dst; int lt0; };
DI KV kv_setup(const char* ws, unsigned lds0, int wid) {
  const int bid = blockIdx.x, idx = bid >> 3, hd = 2 * (bid & 7) + (idx & 1), rb = idx >> 1;
  KV k;
  k.kvb = ws + WS_KV + hd * (64 * 4096) + (wid >> 2) * (32 * 4096) + (wid & 1) * 1024;
  k.lt0 = ((wid >> 1) & 1) + 2 * rb;
  k.dst = lds0 + wid * 1024;
  k.pre = ws + (wid < 4 ? WS_KV + hd * (64 * 4096) + (wid >> 1) * (32 * 4096) + (((wid & 1) + 2 * rb) & 31) * 4096 : WS_Q + (hd * 64 + rb * 4 + (wid - 4)) * 2048);
  asm volatile("" : "+s"(k.kvb), "+s"(k.pre), "+s"(k.dst), "+s"(k.lt0));
  return k;
}
DI constexpr int vpos(int u) { return u < 3 ? VX + u * 8192 : VRING + (u % 3) * 8192; }
DI void k_unit(const KV& k, unsigned l16, int s) { glds16_dev(k.kvb + (((2 * s + 2 + k.lt0) & 31) << 12), l16, k.dst + KRING + (s % 3) * 8192); }
DI void v_unit(const KV& k, unsigned l16, int u) { glds16_dev(k.kvb + (((2 * u + k.lt0) & 31) << 12) + 2048, l16, k.dst + vpos(u)); }
DI void p2_first(const KV& k, unsigned lds0, int wid, unsigned l16, int j) {
  if (j >= 2) k_unit(k, l16, j - 2);
  else glds16_dev(k.pre + j * 1024, l16, lds0 + VRING + wid * 2048 + j * 1024);
}
DI void set_qkv_flag(rsrc_t rs, unsigned epoch) {
  if (threadIdx.x < NREP) st4(rs, F_QKV + 1024 * threadIdx.x + 4 * blockIdx.x, epoch);
}

DI unsigned touch(const Args& A, int wid, int lane, int r) {
  const int w = blockIdx.x * 8 + wid;
  unsigned v;
  if (r == 0) {
    const int row = (w - 1792) * 2 + ((lane >> 3) & 1), l = lane & 7;
    asm volatile("global_load_dword %0, %1, %2" : "=v"(v) : "v"((row < 448 ? row : 447) * QROW + (14 + (l < 7 ? l : 6)) * 128), "s"(A.wqkv) : "memory");
  } else {
    const int lo = 2 * w + (lane & 1), l7 = lane & 7;
    const char* p = (lane & 8) ? A.wo + (lo < 3136 ? lo : 3135) * 128 : A.wd + (7 * w + (l7 < 7 ? l7 : 6)) * 128;
    asm volatile("global_load_dword %0, %1, off" : "=v"(v) : "v"(p) : "memory");
  }
  return v;
}
DI unsigned load_gen(const Args& A) {
  unsigned v;
  asm volatile("global_load_dword %0, %1, %2 sc1" : "=v"(v) : "v"(0), "s"(A.ws + F_GEN) : "memory");
  return v;
}
DI unsigned epoch_of(unsigned genv) { return __builtin_amdgcn_readfirstlane(genv) * 1664525u + 1013904223u; }

DI int krot(int rb, int cgp) { return cgp < 14 ? (cgp + 2 * rb + (rb >> 2)) % 7 : 0; }
DI int kch(int s, int rot) { const int c = s + rot; return c >= 7 ? c - 7 : c; }

DI constexpr int posA(int s) { return s == 0 || s == 6 ? 139264 : 16384 + (s - 1) * 24576; }
DI unsigned phase1_gu(const Args& A, rsrc_t rs, char* lds, int rb, int cgp, unsigned& tvw, KV& kv STAMP_ARG) {
  const int tid = threadIdx.x, lane = tid & 63, wid = __builtin_amdgcn_readfirstlane(tid >> 6);
  const int hh = lane >> 5, l31 = lane & 31, rsb = wid & 3, cg = wid >> 2;
  const int q4 = (lane & 15) >> 2, p4 = lane & 3, blk = (lane >> 4) & 1;
  const unsigned lds0 = (unsigned)(uintptr_t)lds;
  const lds_cptr L = (lds_cptr)(LAS char*)(uintptr_t)lds0;
  const int r8 = lane >> 3, c8 = lane & 7;
  const int xr0 = 16 * wid + r8;
  const char* xs = A.x + (size_t)(128 * rb + 16 * wid) * XROW;
  const unsigned xc0 = r8 * XROW + 16 * (c8 ^ ((xr0 >> 1) & 7)), xc1 = (r8 + 8) * XROW + 16 * (c8 ^ (((xr0 + 8) >> 1) & 7));
  const int sw0 = 32 * ((c8 >> 1) ^ (r8 & 3)) + 16 * (c8 & 1);
  const unsigned oq = r8 * QROW + sw0, og = r8 * FROW + sw0, ou = og ^ 32;
  const char* wq = A.wqkv + (size_t)(8 * wid) * QROW + 128 * cgp;
  const char* wg = A.wg + (size_t)(8 * wid) * FROW + 256 * cgp;
  const char* wu = A.wu + (size_t)(8 * wid) * FROW + 256 * cgp;
  const int rot = krot(rb, cgp);
  auto pieceA = [&](int s, int p) {
    const unsigned sb = lds0 + posA(s);
    const int c = kch(s, rot);
    if (p == 0) glds16(xs + 128 * c, xc0, sb + 2048 * wid);
    if (p == 1) glds16(xs + 128 * c, xc1, sb + 2048 * wid + 1024);
    if (p == 2) glds16(wq + c * (64 * QROW), oq, sb + 16384 + 1024 * wid);
  };
  auto stageA = [&](int s) { pieceA(s, 0); pieceA(s, 1); pieceA(s, 2); };
  auto pieceU = [&](int u, int c, int p) {
    glds16((p ? wu : wg) + kch(c, rot) * (64 * FROW) + 128 * u, p ? ou : og, lds0 + c * 16384 + wid * 2048 + p * 1024);
  };
  STAMP_F(A.ws, 32);
  pieceA(0, 2);
  unsigned genv;
  stageA(1); pieceU(0, 0, 0); pieceU(0, 0, 1); stageA(2);
  STAMP_F(A.ws, 35);

  const int rt32 = rb * 4 + rsb;
  const int xrow = 32 * rsb + l31, xsw = (xrow >> 1) & 7;
  lds_cptr xb[4];
#pragma unroll
  for (int j = 0; j < 4; ++j) xb[j] = L + xrow * 128 + 16 * (((j << 1) | hh) ^ xsw);
  const lds_cptr wt0 = L + 16384 + (8 * hh + q4) * 128 + 8 * p4 + 32 * ((2 * cg + blk) ^ q4);
  const int rowb = hh * 2048 + blk * 1024 + q4 * 128 + 8 * p4;
  const lds_cptr wb0 = L + rowb + 32 * ((2 * cg) ^ q4 ^ blk), wb1 = L + rowb + 32 * ((2 * cg + 1) ^ q4 ^ blk);
  const bool isq = cgp < 7;
  const unsigned hs = isq ? 2048u : 4096u, pad = isq ? 0u : 0x3f80u;
  unsigned qo[2]; bool str[2];
#pragma unroll
  for (int t = 0; t < 2; ++t) {
    const int P = 8 * (isq ? cgp : cgp - 7) + 4 * cg + 2 * t + hh;
    const int hp = (P * 37) >> 8, r = P - 7 * hp;
    const int head = 2 * hp + (r > 3 ? 1 : 0), piece = r > 3 ? r - 4 : r;
    qo[t] = (isq ? WS_Q : WS_KV) + (head * 64 + rt32) * hs + l31 * 16 + piece * 512;
    str[t] = r == 3;
    PIN(qo[t]);
  }
  const float qs = isq ? QSCALE : 1.f;
  unsigned hbase = WS_H + (rt32 * 112 + 8 * cgp + 2 * cg) * 1024 + lane * 16;
  PIN(hbase);
  kv = kv_setup(A.ws, lds0, wid);

  bf16x8 xf[28];
  f32x16 a0, a1, a2, b0, b1, zero16;
#pragma unroll
  for (int i = 0; i < 16; ++i) zero16[i] = 0.f;
  bf16x8 wf[4];
  lds_cptr c0, c1;
  auto rdG = [&](auto Q) {
    constexpr int q = decltype(Q)::v;
    if constexpr (q < 56) {
      constexpr int i = q >> 1;
      if constexpr (q % 28 == 0) { c0 = wb0 + i * 4096; c1 = wb1 + i * 4096; PIN(c0); PIN(c1); }
      const lds_cptr b = (q & 1) ? c1 : c0;
      wf[(q + 2) & 3] = cat8(trread(b + (i % 14) * 4096), trread(b + (i % 14) * 4096 + 512));
    } else if constexpr (q < 112) {
      constexpr int kk = (q - 56) % 28;
      if constexpr (kk % 14 == 0) { c0 = (q < 84 ? wb0 : wb1) + kk * 4096; PIN(c0); }
      wf[(q + 2) & 3] = cat8(trread(c0 + (kk % 14) * 4096), trread(c0 + (kk % 14) * 4096 + 512));
    }
  };

  STAMP_F(A.ws, 36);
  {
    bf16x8 fq[2][4];
    sfor<0, 8>([&](auto S) {
      constexpr int s = decltype(S)::v;
      if constexpr (s < 7) {
        waitv_bar<(s == 1 ? 9 : s < 3 ? 8 : s + 6)>();
        if constexpr (s == 0) STAMP_AT(A.ws, 1); else STAMP_F(A.ws, 11 + 2 * s);
        lds_cptr cq = wt0 + posA(s);
        PIN(cq);
#pragma unroll
        for (int j = 0; j < 4; ++j) {
          xf[4 * s + j] = ldsread16(xb[j] + posA(s));
          fq[s & 1][j] = cat8(trread(cq + j * 2048), trread(cq + j * 2048 + 512));
        }
      } else { rdG(IC<0>{}); rdG(IC<1>{}); rdG(IC<2>{}); }
      SBAR();
      sfor<0, 4>([&](auto J) {
        constexpr int j = decltype(J)::v, i = 4 * (s - 1) + j, n = 4 * s + j;
        if constexpr (s > 0) a0 = MFMA32(fq[(s - 1) & 1][j], xf[i], i == 0 ? zero16 : a0);
        SBAR();
        if constexpr (n < 12) pieceA(3 + n / 3, n % 3);
        else if constexpr (n == 12) genv = load_gen(A);
        else if constexpr (n < 25) pieceU(0, 1 + (n - 13) / 2, (n - 13) & 1);
        SBAR();
      });
    });
  }
  STAMP_AT(A.ws, 2);
  unsigned epoch;
  u32x4 qk[2];
  float hv[8];
  auto poll = [&]() { glds16_dev(A.ws + F_QKV + 1024 * (blockIdx.x & (NREP - 1)), lane * 16, lds0 + VRING + wid * 2048); };
  sfor<0, 112>([&](auto Q) {
    constexpr int q = decltype(Q)::v, i = q - 56;
    if constexpr (q < 64 && (q & 7) == 5) {
      constexpr int c = q >> 3;
      waitv_bar_nl<(c == 0 ? 12 : c == 1 ? 10 : c == 2 ? 10 : c == 3 ? 4 : c == 6 ? 6 : c == 7 ? 4 : 20)>();
      if constexpr (c == 0) { PIN(genv); epoch = epoch_of(genv); }
      if constexpr (c == 3) { STAMP_F(A.ws, 27); set_qkv_flag(rs, epoch); }
      if constexpr (c == 6) STAMP_F(A.ws, 29);
    }
    if constexpr (i == 13) { waitv_bar_nl<2>(); STAMP_F(A.ws, 30); }
    if constexpr (i == 21) { waitv_bar_nl<1>(); STAMP_F(A.ws, 31); }
    if constexpr (i == QKV_STEP) STAMP_F(A.ws, 37);
    if constexpr (i == QKV_STEP) {
      if (wid == 0) {
        asm volatile("s_waitcnt vmcnt(0)" ::: "memory");
        const u32x4 f = __builtin_bit_cast(u32x4, ldsread16(L + VRING + lane * 16));
        if (!__all(lane >= 56 || (f[0] == epoch && f[1] == epoch && f[2] == epoch && f[3] == epoch))) wait_flags256(rs, F_QKV + 1024 * (blockIdx.x & (NREP - 1)), epoch, lane);
      }
      waitv_bar_nl<20>();
      STAMP_F(A.ws, 38);
    }
    rdG(IC<q + 3>{});
    SBAR();
    if constexpr (q < 56) {
      if constexpr (q & 1) a2 = MFMA32(wf[(q + 2) & 3], xf[q >> 1], q < 2 ? zero16 : a2);
      else a1 = MFMA32(wf[(q + 2) & 3], xf[q >> 1], q < 2 ? zero16 : a1);
    } else if constexpr (i < 28) b0 = MFMA32(wf[(q + 2) & 3], xf[i], i == 0 ? zero16 : b0);
    else b1 = MFMA32(wf[(q + 2) & 3], xf[i - 28], i == 28 ? zero16 : b1);
    SBAR();
    if constexpr (q >= 13 && q < 64 && ((q & 7) == 5 || (q & 7) == 6)) pieceU(1, (q >> 3) - 1, (q & 7) - 5);
    if constexpr (q == 0 || q == 2) {
      constexpr int t = q >> 1;
      const unsigned w0 = pk2(a0[8 * t] * qs, a0[8 * t + 1] * qs), w1 = pk2(a0[8 * t + 2] * qs, a0[8 * t + 3] * qs);
      const unsigned w2 = pk2(a0[8 * t + 4] * qs, a0[8 * t + 5] * qs), w3 = pk2(a0[8 * t + 6] * qs, a0[8 * t + 7] * qs);
      auto r0 = __builtin_amdgcn_permlane32_swap(w0, w2, false, false);
      auto r1 = __builtin_amdgcn_permlane32_swap(w1, w3, false, false);
      qk[t] = u32x4{r0[0], r1[0], r0[1], r1[1]};
      PIN(qk[t]);
    }
    if constexpr (q == 1 || q == 3) {
      constexpr int t = q >> 1;
      u32x4 v = qk[t];
      if (str[t]) { v[2] = pad; v[3] = 0u; }
      st16(rs, qo[t], v);
      if (str[t]) st16(rs, qo[t] + 64 * hs, u32x4{qk[t][2], qk[t][3], pad, 0u});
    }
    if constexpr (i >= 1 && i < 52 && (i - 1) % 17 < 16) {
      constexpr int t = (i - 1) / 17, e = ((i - 1) % 17) >> 1;
      const f32x16& a = t == 0 ? a1 : t == 1 ? a2 : b0;
      if constexpr (((i - 1) % 17) & 1) hv[e] = a[e + 8] * __builtin_amdgcn_rcpf(1.f + hv[e]);
      else hv[e] = EX2(-LOG2E * a[e]);
      PIN(hv[e]);
    }
    if constexpr (i == 17) st16(rs, hbase, pack8(hv));
    if constexpr (i == 34) st16(rs, hbase + 1024, pack8(hv));
    if constexpr (i == 51) st16(rs, hbase + 4096, pack8(hv));
    if constexpr (i == 35) tvw = touch(A, wid, lane, 4);
    if constexpr (i == 0) { if (wid == 0) poll(); }
    if constexpr (i >= QKV_STEP && i < QKV_STEP + 4) p2_first(kv, lds0, wid, lane * 16, i - QKV_STEP);
    SBAR();
  });
#pragma unroll
  for (int i = 0; i < 8; ++i) hv[i] = sigm_mul(b1[i], b1[i + 8]);
  st16(rs, hbase + 5120, pack8(hv));
  asm volatile("s_waitcnt vmcnt(4)" ::: "memory");
  STAMP_AT(A.ws, 3);
  return epoch;
}

DI unsigned phase1_v(const Args& A, rsrc_t rs, char* lds, int rb, int cgp, unsigned& tvw, KV& kv STAMP_ARG) {
  const int tid = threadIdx.x, lane = tid & 63, wid = __builtin_amdgcn_readfirstlane(tid >> 6);
  const int hh = lane >> 5, l31 = lane & 31, rsb = wid & 3, cg = wid >> 2;
  const int q4 = (lane & 15) >> 2, p4 = lane & 3, blk = (lane >> 4) & 1;
  const unsigned lds0 = (unsigned)(uintptr_t)lds;
  const lds_cptr L = (lds_cptr)(LAS char*)(uintptr_t)lds0;
  const int r8 = lane >> 3, c8 = lane & 7;
  const int xr0 = 16 * wid + r8;
  const char* xs = A.x + (size_t)(128 * rb + 16 * wid) * XROW;
  const unsigned xc0 = r8 * XROW + 16 * (c8 ^ ((xr0 >> 1) & 7)), xc1 = (r8 + 8) * XROW + 16 * (c8 ^ (((xr0 + 8) >> 1) & 7));
  const unsigned oq = r8 * QROW + 32 * ((c8 >> 1) ^ (r8 & 3)) + 16 * (c8 & 1);
  const int l0 = cgp == 14 ? 14 : 18;
  const char* wa = A.wqkv + (size_t)(8 * wid) * QROW + 128 * l0;
  const char* wbs = A.wqkv + 128 * 17 + 64 * (cgp - 14);
  auto piece = [&](int s, int p) {
    if (s < 7) {
      const unsigned sb = lds0 + pos1(s);
      if (p == 0) glds16(xs + 128 * s, xc0, sb + 2048 * wid);
      if (p == 1) glds16(xs + 128 * s, xc1, sb + 2048 * wid + 1024);
      if (p >= 2) glds16(wa + (size_t)(64 * s) * QROW + 128 * (p - 2), oq, sb + 16384 + 8192 * (p - 2) + 1024 * wid);
    }
  };
  auto piece17 = [&](int p) {
    const int g = wid < 7 ? 4 * wid + p : 24 + p;
    glds16(wbs + (size_t)(16 * g) * QROW, (lane >> 2) * QROW + (lane & 3) * 16, lds0 + 122880 + g * 1024);
  };
#pragma unroll
  for (int p = 2; p < 5; ++p) piece(0, p);
  unsigned tv0 = touch(A, wid, lane, 0), genv;
#pragma unroll
  for (int p = 0; p < 5; ++p) piece(1, p);
  const int rt32 = rb * 4 + rsb;
  const int xrow = 32 * rsb + l31, xsw = (xrow >> 1) & 7;
  lds_cptr xb[4];
#pragma unroll
  for (int j = 0; j < 4; ++j) xb[j] = L + xrow * 128 + 16 * (((j << 1) | hh) ^ xsw);
  const lds_cptr wt0 = L + 16384 + (8 * hh + q4) * 128 + 8 * p4 + 32 * ((2 * cg + blk) ^ q4);
  lds_cptr wb0 = L + 122880 + (8 * hh + q4) * 64 + 32 * blk + 8 * p4;
  PIN(wb0);
  unsigned vo[4];
#pragma unroll
  for (int t = 0; t < 4; ++t) {
    const int tile = t < 3 ? 2 * l0 + 2 * t + cg : 34 + cgp - 14;
    const int n = 32 * (tile - 28) + l31, head = (n * 2341) >> 16, dh = n - 28 * head;
    vo[t] = WS_KV + ((head * 64 + rt32) * 4 + 2) * 1024 + (dh + 32 * hh) * 16;
    PIN(vo[t]);
  }
  kv = kv_setup(A.ws, lds0, wid);
  bf16x8 xf[28];
  f32x16 a0, a1, a2, b0, zero16;
#pragma unroll
  for (int i = 0; i < 16; ++i) zero16[i] = 0.f;
  bf16x8 wf[6];
  lds_cptr cx[4], c0;
  auto rdA = [&](auto I, auto M) {
    constexpr int i = decltype(I)::v, m = decltype(M)::v, s = i >> 2, j = i & 3, w = (i & 1) * 3;
    if constexpr (m == 0 && j == 0) {
#pragma unroll
      for (int t = 0; t < 4; ++t) { cx[t] = xb[t] + pos1(s); PIN(cx[t]); }
      c0 = wt0 + pos1(s); PIN(c0);
    }
    if constexpr (m == 0) xf[i] = ldsread16(cx[j]);
    wf[w + m] = cat8(trread(c0 + m * 8192 + j * 2048), trread(c0 + m * 8192 + j * 2048 + 512));
  };
  auto rdB = [&](auto I) {
    constexpr int i = decltype(I)::v;
    wf[i & 3] = cat8(trread(wb0 + i * 1024), trread(wb0 + i * 1024 + 256));
  };
  waitv_bar<6>();
  STAMP_AT(A.ws, 1);
  rdA(IC<0>{}, IC<0>{}); rdA(IC<0>{}, IC<1>{}); rdA(IC<0>{}, IC<2>{});
  sfor<0, 28>([&](auto I) {
    constexpr int i = decltype(I)::v, s = i >> 2, w = (i & 1) * 3;
    auto vm = [&](auto M) {
      constexpr int m = decltype(M)::v, r = i & 3;
      if constexpr (m == 0) piece(s + 2, r == 0 ? 0 : r + 1);
      if constexpr (m == 1 && r == 0) piece(s + 2, 1);
      if constexpr (i == 11 && m == 1) genv = load_gen(A);
      if constexpr (i >= 20 && i < 22 && m < 2) piece17(2 * (i - 20) + m);
      if constexpr (i == 22 && m == 0) glds16(A.gw, (lane < 56 ? lane : 55) * 16, lds0 + 151552 + 1024 * wid);
    };
    auto rd = [&](auto M) { if constexpr (i < 27) rdA(IC<i + 1>{}, M); else rdB(M); };
    SBAR();
    a0 = MFMA32(xf[i], wf[w], i == 0 ? zero16 : a0);
    if constexpr ((i & 3) == 3) {
      waitv_bar<(s == 3 ? 5 : s == 5 ? 5 : s == 6 ? 0 : 4)>();
      STAMP_F(A.ws, 13 + 2 * s);
      if constexpr (s == 0) PIN(tv0);
      if constexpr (s == 4) PIN(genv);
    }
    SBAR(); rd(IC<0>{}); vm(IC<0>{}); SBAR();
    a1 = MFMA32(xf[i], wf[w + 1], i == 0 ? zero16 : a1);
    SBAR(); rd(IC<1>{}); vm(IC<1>{}); SBAR();
    a2 = MFMA32(xf[i], wf[w + 2], i == 0 ? zero16 : a2);
    SBAR(); rd(IC<2>{}); vm(IC<2>{}); SBAR();
  });
  STAMP_AT(A.ws, 2);
  const unsigned epoch = epoch_of(genv);
  auto vstore = [&](const f32x16& a, unsigned o) {
    float v[16];
#pragma unroll
    for (int i = 0; i < 16; ++i) v[i] = a[i];
    st16(rs, o, pack8(v)); st16(rs, o + 1024, pack8(v + 8));
  };
  {
    const int head = 8 * (cgp - 14) + 4 * cg + (lane >> 4);
    const unsigned o = WS_KV + ((head * 64 + rt32) * 4 + 2 + ((lane >> 3) & 1)) * 1024 + (28 + (lane & 3) + 32 * ((lane >> 2) & 1)) * 16;
    const unsigned v = (lane & 3) == 0 ? 0x3f803f80u : 0u;
    st16(rs, o, u32x4{v, v, v, v});
  }
  if (cg == 0) {
    sfor<0, 28>([&](auto I) {
      constexpr int i = decltype(I)::v;
      if constexpr (i + 3 < 28) rdB(IC<i + 3>{});
      SBAR();
      b0 = MFMA32(xf[i], wf[i & 3], i == 0 ? zero16 : b0);
      if constexpr (i == 2) vstore(a0, vo[0]);
      if constexpr (i == 5) vstore(a1, vo[1]);
      if constexpr (i == 9) vstore(a2, vo[2]);
      SBAR();
    });
    vstore(b0, vo[3]);
    STAMP_F(A.ws, 27);
  } else {
    vstore(a0, vo[0]); vstore(a1, vo[1]); vstore(a2, vo[2]);
    if (wid == 4) wait_flags256(rs, F_QKV + 1024 * (blockIdx.x & (NREP - 1)), epoch, lane);
  }
  waitv_bar<0>();
  STAMP_F(A.ws, 29);
  set_qkv_flag(rs, epoch);
#pragma unroll
  for (int j = 0; j < 4; ++j) p2_first(kv, lds0, wid, lane * 16, j);
  if (cg == 1 && cgp == 14) {
    const lds_cptr gp = L + 151552 + 1024 * wid + 16 * hh;
    f32x16 ga = zero16;
#pragma unroll
    for (int i = 0; i < 28; ++i) ga = MFMA32(ldsread16(gp + 32 * i), xf[i], ga);
    asm volatile("s_waitcnt vmcnt(0)" ::: "memory");
    const float g = rbf(1.f / (1.f + __expf(-rbf(ga[0]))));
    if (lane < 32) st4(rs, WS_G + 4 * (32 * rt32 + lane), __float_as_uint(g));
  } else asm volatile("s_waitcnt vmcnt(0)" ::: "memory");
  STAMP_AT(A.ws, 3);
  asm volatile("fused_code_end2:" ::: "memory");
  return epoch;
}

DI float xhalf_max(float v) {
  auto r = __builtin_amdgcn_permlane32_swap(__float_as_uint(v), __float_as_uint(v), false, false);
  return fmaxf(__uint_as_float(r[0]), __uint_as_float(r[1]));
}
DI float max16(const f32x16& s) {
  float a = fmaxf(fmaxf(s[0], s[1]), s[2]), b = fmaxf(fmaxf(s[3], s[4]), s[5]);
  a = fmaxf(fmaxf(a, s[6]), s[7]); b = fmaxf(fmaxf(b, s[8]), s[9]);
  a = fmaxf(fmaxf(a, s[10]), s[11]); b = fmaxf(fmaxf(b, s[12]), s[13]);
  a = fmaxf(fmaxf(a, s[14]), s[15]);
  return fmaxf(a, b);
}

DI void phase23(const Args& A, rsrc_t rs, char* lds, int rb1, int cgp, unsigned epoch, unsigned tvw, const KV& kv STAMP_ARG) {
  const int bid = blockIdx.x, xcd = bid & 7, idx = bid >> 3;
  const int hd = 2 * xcd + (idx & 1), rb = idx >> 1;
  const int i3 = idx < 28 ? idx : idx - 28, rt64 = 4 * xcd + i3 / 7, c7 = i3 % 7;
  const int tid = threadIdx.x, lane = tid & 63, wid = __builtin_amdgcn_readfirstlane(tid >> 6);
  const int hh = lane >> 5, qs = wid & 3, kh = wid >> 2;
  const unsigned lds0 = (unsigned)(uintptr_t)lds;
  const lds_cptr L = (lds_cptr)(LAS char*)(uintptr_t)lds0;
  const unsigned kvo = WS_KV + hd * (64 * 4096) + kh * (32 * 4096);
  const unsigned l16 = lane * 16;
  const int r3 = wid & 1, kq = wid >> 1;
  const int r8 = lane >> 3, c8 = lane & 7;
  const unsigned ow = r8 * XROW + 32 * ((c8 >> 1) ^ (r8 & 3)) + 16 * (c8 & 1);
  const char* wds = A.wd + (size_t)(448 * kq + 16 * r3) * XROW + 128 * c7;
  auto stage_wd = [&](int c, int slot, int p) {
    glds16(wds + (size_t)(32 * c + 8 * p) * XROW, ow, lds0 + slot * 16384 + wid * 2048 + p * 1024);
  };

  waitv_bar<20>();
  STAMP_AT(A.ws, 4);
  const unsigned fvo = F_QKV + 1024 * (bid & (NREP - 1)) + 896;
  unsigned fv = look16(A.ws + fvo, wid, lane, 31);
  const lds_cptr pb = L + VRING + l16;
  const bf16x8 q0 = ldsread16(pb + 8192 + qs * 2048), q1 = ldsread16(pb + 8192 + qs * 2048 + 1024);
  const bf16x8 ka0 = ldsread16(pb + kh * 4096), ka1 = ldsread16(pb + kh * 4096 + 1024);
  bf16x8 kf0 = ldsread16(pb + kh * 4096 + 2048), kf1 = ldsread16(pb + kh * 4096 + 3072);
  f32x16 zero16;
#pragma unroll
  for (int i = 0; i < 16; ++i) zero16[i] = 0.f;
  f32x16 sA = MFMA32(ka0, q0, zero16);
  float mref;
  {
    f32x16 s = MFMA32(ka1, q1, sA);
    mref = rbf(xhalf_max(max16(s)));
  }
  bf16x8 q1m = q1;
  if (hh) q1m[4] = (short)((__float_as_uint(-mref)) >> 16);
  sA = MFMA32(ka1, q1m, sA);
  PIN(kf0); PIN(kf1);
  STAMP_AT(A.ws, 5);
  f32x16 o = zero16, sB;
  bf16x8 vf0, vf1, xf0, xf1;
  u32x4 P0[VLAG + 1], P1[VLAG + 1];
  float e0 = 0.f, e1 = 0.f, e2 = 0.f, e3 = 0.f;
  lds_cptr rk = L + KRING + kh * 4096 + l16, rx = L + VX + kh * 4096 + l16;
  PIN(rk); PIN(rx);
  bf16x8 hf[28];
  unsigned fh;
  float g;
  const unsigned ho = WS_H + ((rt64 * 2 + r3) * 112 + 28 * kq) * 1024 + lane * 16;
#define B8(x) __builtin_bit_cast(bf16x8, x)
#define G(MF, RDS, W0, W1, C, i) do { MF; SBAR(); RDS; SBAR(); \
    float n0 = EX2(C[i]); PIN(n0); SBAR(); W0 = pk2(e0, e1); PIN(W0); SBAR(); float n1 = EX2(C[i + 1]); PIN(n1); SBAR(); W1 = pk2(e2, e3); PIN(W1); SBAR(); \
    float n2 = EX2(C[i + 2]); PIN(n2); float n3 = EX2(C[i + 3]); PIN(n3); e0 = n0; e1 = n1; e2 = n2; e3 = n3; SBAR(); } while (0)
  sfor<0, 16>([&](auto S) {
    constexpr int s = decltype(S)::v;
    constexpr auto D = [](int t) { return t < 0 ? 0 : (t + 2 <= 14 ? 1 : 0) + (t >= 1 && t + 2 <= 15 ? 1 : 0) + (t >= 1 && t <= 3 ? 1 : 0); };
    constexpr auto E = [](int t) { return t < 0 ? 0 : (t < 5 || t == 6 || t == 7) ? 2 : (t >= 8 && t < 15) ? 4 : 0; };
    constexpr int N = s == 0 ? 20 : E(s - 2) + D(s - 1) + E(s - 1);
    if constexpr (s == 1) {
      asm volatile("s_waitcnt vmcnt(3)" : "+v"(fv)::"memory");
      if (wid == 0 && !__all(fv == epoch)) wait_flags(rs, fvo, 32, epoch, lane);
    }
    if constexpr (s == 8) {
      PIN(fh);
      if (wid == 0 && !__all(fh == epoch)) wait_flags(rs, F_H + 128 * (rt64 >> 1), 16, epoch, lane);
    }
    waitv_bar<N>();
    if constexpr (s == 1) { STAMP_F(A.ws, 39); PIN(tvw); }
    if constexpr (s == 1) { if (tid == 0) st4(rs, F_H + 128 * rb1 + 4 * cgp, epoch); }
    if constexpr (s == 5) fh = look16(A.ws + F_H + 128 * (rt64 >> 1), wid, lane);
    auto vm = [&](auto J) {
      constexpr int j = decltype(J)::v, d = D(s), e = j - d;
      if constexpr (j < d) {
        if constexpr (j == 0 && s + 2 <= 14) k_unit(kv, l16, s + 2);
        else if constexpr (j == 2) v_unit(kv, l16, s - 1);
        else v_unit(kv, l16, s + 2);
      } else if constexpr (e < E(s)) {
        if constexpr (s < 8) stage_wd(s < 5 ? s : s - 1, s < 5 ? s : s - 1, e);
        else hf[4 * (s - 8) + e] = ldfrag(rs, ho + (4 * (s - 8) + e) * 1024);
      } else if constexpr (s == 15 && j == 2) g = __uint_as_float(ld4(rs, WS_G + 4 * (rt64 * 64 + r3 * 32 + (lane & 31))));
    };
    SBAR();
    sfor<0, 2>([&](auto T) {
      constexpr int t = decltype(T)::v, i = 2 * s + t;
      constexpr int pc = i < VLAG ? i : VLAG, pp = i - 1 < VLAG ? (i > 0 ? i - 1 : VLAG) : VLAG;
      constexpr bool pv = i >= VLAG, cu = i >= VLAG && i < 2 * VLAG;
      constexpr int ko = (s % 3) * 8192 + t * 2048, vo = 24576 + ko, xo = ((s - 3) % 3) * 8192 + t * 2048;
      f32x16& C = t ? sB : sA;
      f32x16& Nx = t ? sA : sB;
      __builtin_amdgcn_s_setprio(3 - 2 * t);
      G(if constexpr (i < 31) Nx = MFMA32(kf0, q0, zero16),
        if constexpr (cu) { xf0 = ldsread16(rx + xo); xf1 = ldsread16(rx + xo + 1024); } vm(IC<3 * t>{}), P1[pp][2], P1[pp][3], C, 0);
      G(if constexpr (i > VLAG) o = MFMA32(vf1, B8(P1[VLAG]), o),
        if constexpr (pv) vf0 = ldsread16(rk + vo); vm(IC<3 * t + 1>{}), P0[pc][0], P0[pc][1], C, 4);
      if constexpr (cu) { o = MFMA32(xf0, B8(P0[i - VLAG]), o); SBAR(); }
      __builtin_amdgcn_s_setprio(2 - 2 * t);
      G(if constexpr (i < 31) Nx = MFMA32(kf1, q1m, Nx),
        if constexpr (pv) vf1 = ldsread16(rk + vo + 1024); if constexpr (i < 30) { kf0 = ldsread16(rk + ko); kf1 = ldsread16(rk + ko + 1024); }, P0[pc][2], P0[pc][3], C, 8);
      G(if constexpr (pv) o = MFMA32(vf0, B8(P0[VLAG]), o), vm(IC<3 * t + 2>{}), P1[pc][0], P1[pc][1], C, 12);
      if constexpr (cu) { o = MFMA32(xf1, B8(P1[i - VLAG]), o); SBAR(); }
    });
  });
  P1[VLAG][2] = pk2(e0, e1); P1[VLAG][3] = pk2(e2, e3);
  o = MFMA32(vf1, B8(P1[VLAG]), o);
#undef G
  STAMP_AT(A.ws, 6);
  {
    float t0 = fabsf(o[0]) + fabsf(o[1]), t1 = fabsf(o[2]) + fabsf(o[3]);
#pragma unroll
    for (int i = 4; i < 16; i += 4) { t0 += fabsf(o[i]) + fabsf(o[i + 1]); t1 += fabsf(o[i + 2]) + fabsf(o[i + 3]); }
    const bool bad = !(t0 + t1 <= 1.0e30f);
    if (__builtin_expect(__any(bad), 0)) {
      float m = -INFINITY;
      o = zero16;
#pragma unroll 1
      for (int t = 0; t < 32; ++t) {
        const unsigned p = kvo + t * 4096 + lane * 16;
        f32x16 s = MFMA32(ldfrag(rs, p), q0, zero16);
        s = MFMA32(ldfrag(rs, p + 1024), q1, s);
        const float mn = fmaxf(m, xhalf_max(max16(s)));
        const float al = EX2(m - mn);
        m = mn;
        float e[16];
#pragma unroll
        for (int i = 0; i < 16; ++i) { o[i] *= al; e[i] = EX2(s[i] - mn); }
        o = MFMA32(ldfrag(rs, p + 2048), __builtin_bit_cast(bf16x8, pack8(e)), o);
        o = MFMA32(ldfrag(rs, p + 3072), __builtin_bit_cast(bf16x8, pack8(e + 8)), o);
      }
      mref = m;
    }
  }
  {
    auto lr = __builtin_amdgcn_permlane32_swap(__float_as_uint(o[12]), __float_as_uint(o[12]), false, false);
    const float lown = __uint_as_float(lr[1]);
    f32x4* xs = (f32x4*)(lds + KRING) + wid * (3 * 64) + lane;
    const f32x4* xr = (const f32x4*)(lds + KRING) + (wid ^ 4) * (3 * 64) + lane;
    f32x4 k0, k1;
    if (kh == 0) { xs[0] = f32x4{o[8], o[9], o[10], o[11]}; xs[64] = f32x4{o[12], o[13], o[14], o[15]}; k0 = f32x4{o[0], o[1], o[2], o[3]}; k1 = f32x4{o[4], o[5], o[6], o[7]}; }
    else { xs[0] = f32x4{o[0], o[1], o[2], o[3]}; xs[64] = f32x4{o[4], o[5], o[6], o[7]}; k0 = f32x4{o[8], o[9], o[10], o[11]}; k1 = f32x4{o[12], o[13], o[14], o[15]}; }
    xs[128] = f32x4{mref, lown, 0.f, 0.f};
    waitv_bar<0>();
    PIN(g);
    const f32x4 t0 = xr[0], t1 = xr[64], ml = xr[128];
    const float M = fmaxf(mref, ml[0]);
    float a0 = EX2(mref - M), a1 = EX2(ml[0] - M);
    const float inv = 1.f / (lown * a0 + ml[1] * a1);
    a0 *= inv; a1 *= inv;
    float v[8];
#pragma unroll
    for (int j = 0; j < 4; ++j) { v[j] = k0[j] * a0 + t0[j] * a1; v[4 + j] = k1[j] * a0 + t1[j] * a1; }
    if (kh && hh) { v[4] = 0.f; v[5] = 0.f; v[6] = 0.f; v[7] = 0.f; }
    st16(rs, WS_O + (((rb * 4 + qs) * 16 + hd) * 2 + kh) * 1024 + l16, pack8(v));
  }
  STAMP_AT(A.ws, 7);
  if (idx >= 28) {
    waitv_bar<0>();
    if (tid == 0) st4(rs, F_O + 128 * rb + 4 * hd, epoch);
    if (bid == 255 && tid == 0) st4(rs, F_GEN, epoch);
    return;
  }

  const int q4 = (lane & 15) >> 2, p4 = lane & 3, blk = (lane >> 4) & 1;
  const int wro = (4 * hh + q4) * 128 + 8 * p4;
  const lds_cptr wd0 = L + kq * 4096 + wro + 32 * ((0 + blk) ^ q4), wd1 = L + kq * 4096 + wro + 32 * ((2 + blk) ^ q4);
  f32x16 bc0, bc1;
  bf16x8 wf[4], of[8];
  lds_cptr d0, d1;
  auto rdW = [&](auto Q) {
    constexpr int q = decltype(Q)::v, sl = (q >> 2) % 10, so = (sl % 3) * 16384 + ((q >> 1) & 1) * 2048;
    if constexpr (q < 56) {
      if constexpr ((q & 3) == 0 && sl % 3 == 0) { d0 = wd0 + sl * 16384; d1 = wd1 + sl * 16384; PIN(d0); PIN(d1); }
      const lds_cptr b = (q & 1) ? d1 : d0;
      wf[q & 3] = cat8(trread(b + so), trread(b + so + 1024));
    }
  };
  const char* wos = A.wo + (size_t)(56 * wid) * XROW + 128 * c7;
  const unsigned oo = WS_O + (((rt64 * 2 + r3) * 16 + 4 * kq) * 2) * 1024 + l16;
  unsigned fo;
  rdW(IC<0>{}); rdW(IC<1>{}); rdW(IC<2>{});
  sfor<0, 56>([&](auto Q) {
    constexpr int q = decltype(Q)::v;
    if constexpr (q == 2) waitv_bar_nl<20>();
    if constexpr (q == 18) {
      waitv_bar_nl<6>();
      if (tid == 0) st4(rs, F_O + 128 * rb + 4 * hd, epoch);
    }
    if constexpr (q == 25) waitv_bar_nl<7>();
    if constexpr (q == 34) waitv_bar_nl<20>();
    if constexpr (q == 37) waitv_bar_nl<4>();
    if constexpr (q == O_STEP) {
      STAMP_AT(A.ws, 8);
      asm volatile("s_waitcnt vmcnt(7)" : "+v"(fo)::"memory");
      if (wid == 0 && !__all(fo == epoch)) wait_flags(rs, F_O + 128 * (rt64 >> 1), 16, epoch, lane);
      waitv_bar_nl<20>();
      STAMP_AT(A.ws, 9);
    }
    rdW(IC<q + 3>{});
    SBAR();
    if constexpr (q & 1) bc1 = MFMA32(wf[q & 3], hf[q >> 1], q < 2 ? zero16 : bc1);
    else bc0 = MFMA32(wf[q & 3], hf[q >> 1], q < 2 ? zero16 : bc0);
    SBAR();
    if constexpr (q >= 2 && q < 8) stage_wd(7 + (q - 2) / 2, 7 + (q - 2) / 2, q & 1);
    if constexpr (q >= 18 && q < 26) stage_wd(10 + (q - 18) / 2, (q - 18) / 2, q & 1);
    if constexpr (q == 33) fo = look16(A.ws + F_O + 128 * (rt64 >> 1), wid, lane);
    if constexpr (q >= 34 && q < 41) glds16(wos + (size_t)(8 * (q - 34)) * XROW, ow, lds0 + 65536 + wid * 7168 + (q - 34) * 1024);
    if constexpr (q >= O_STEP && q < O_STEP + 8) of[q - O_STEP] = ldfrag(rs, oo + (q - O_STEP) * 1024);
    SBAR();
  });
  const float gm = rbf(1.f - g);
  lds_cptr wo0 = L + 65536 + kq * (112 * 128) + wro + 32 * ((0 + blk) ^ q4), wo1 = L + 65536 + kq * (112 * 128) + wro + 32 * ((2 + blk) ^ q4);
  lds_cptr wo0h = wo0 + (hh ? 2048 : 3072), wo1h = wo1 + (hh ? 2048 : 3072);
  PIN(wo0); PIN(wo1); PIN(wo0h); PIN(wo1h);
  lds_ptr rp = (lds_ptr)L + (kq < 3 ? kq * (64 * 272) : 122880) + (r3 * 32 + (lane & 31)) * 272 + 16 * hh;
  PIN(rp);
  waitv_bar<8>();
  STAMP_AT(A.ws, 10);
  f32x16 ac0, ac1;
  auto rdO = [&](auto Q) {
    constexpr int q = decltype(Q)::v, b = ((q & 7) >> 1) * 28 * 128;
    if constexpr (q < 16) {
      const lds_cptr w = q < 8 ? wo0 : wo1, wh = q < 8 ? wo0h : wo1h;
      if constexpr (q & 1) wf[q & 3] = cat8(trread(w + b + 2048), trread(wh + b));
      else wf[q & 3] = cat8(trread(w + b), trread(w + b + 1024));
    }
  };
  rdO(IC<0>{}); rdO(IC<1>{}); rdO(IC<2>{});
  sfor<0, 16>([&](auto Q) {
    constexpr int q = decltype(Q)::v;
    rdO(IC<q + 3>{});
    SBAR();
    if constexpr (q < 8) ac0 = MFMA32(wf[q & 3], of[q & 7], q == 0 ? zero16 : ac0);
    else ac1 = MFMA32(wf[q & 3], of[q & 7], q == 8 ? zero16 : ac1);
    SBAR();
    if constexpr (q < 8) {
#pragma unroll
      for (int j = 0; j < 2; ++j) { bc0[2 * q + j] *= gm; bc1[2 * q + j] *= gm; }
      PIN(bc0); PIN(bc1);
    }
    if constexpr (q >= 10 && q < 14) {
      constexpr int i = q - 10;
      *(LAS f32x4*)(rp + 32 * i) = f32x4{g * ac0[4 * i] + bc0[4 * i], g * ac0[4 * i + 1] + bc0[4 * i + 1], g * ac0[4 * i + 2] + bc0[4 * i + 2], g * ac0[4 * i + 3] + bc0[4 * i + 3]};
    }
    SBAR();
  });
#pragma unroll
  for (int i = 0; i < 4; ++i)
    *(LAS f32x4*)(rp + 128 + 32 * i) = f32x4{g * ac1[4 * i] + bc1[4 * i], g * ac1[4 * i + 1] + bc1[4 * i + 1], g * ac1[4 * i + 2] + bc1[4 * i + 2], g * ac1[4 * i + 3] + bc1[4 * i + 3]};
  __syncthreads();
  {
    const int row = tid >> 3, cc = tid & 7;
    const char* rp = lds + row * 272 + cc * 32;
    f32x4 s0 = *(const f32x4*)rp, s1 = *(const f32x4*)(rp + 16);
#pragma unroll
    for (int k = 1; k < 4; ++k) { const int ko = k < 3 ? k * (64 * 272) : 122880; s0 += *(const f32x4*)(rp + ko); s1 += *(const f32x4*)(rp + ko + 16); }
    u32x4 w = {pk2(s0[0], s0[1]), pk2(s0[2], s0[3]), pk2(s1[0], s1[1]), pk2(s1[2], s1[3])};
    const rsrc_t ro = __builtin_amdgcn_make_buffer_rsrc((void*)A.out, 0, 2048 * XROW, 0x00020000);
    st16(ro, (rt64 * 64 + row) * XROW + c7 * 128 + cc * 16, w);
  }
  STAMP_AT(A.ws, 11);
  asm volatile("fused_code_end:");
}

__global__ __launch_bounds__(512, 2) void fused_kernel(Args A) {
  extern __shared__ __attribute__((aligned(16))) char lds[];
  const int bid = blockIdx.x, xcd = bid & 7, idx = bid >> 3;
  const int rb1 = 4 * (xcd & 3) + (idx & 3), cgp = idx < 28 ? 7 * (xcd >> 2) + (idx >> 2) : 14 + (xcd >> 2);
  STAMP_AT(A.ws, 0);
  {
    const int lane = threadIdx.x & 63, wid = __builtin_amdgcn_readfirstlane(threadIdx.x >> 6);
    const int r8 = lane >> 3, c8 = lane & 7, xr0 = 16 * wid + r8;
    const char* xs = A.x + (size_t)(128 * rb1 + 16 * wid) * XROW + 128 * krot(rb1, cgp);
    const unsigned sb = (unsigned)(uintptr_t)lds + (cgp < 14 ? posA(0) : pos1(0)) + 2048 * wid;
    glds16(xs, r8 * XROW + 16 * (c8 ^ ((xr0 >> 1) & 7)), sb);
    glds16(xs, (r8 + 8) * XROW + 16 * (c8 ^ (((xr0 + 8) >> 1) & 7)), sb + 1024);
  }
  unsigned cv = 0;
  {
    unsigned long long pc; unsigned len;
    asm volatile("s_getpc_b64 %0\nfused_code_begin:\n\ts_mov_b32 %1, fused_code_end-fused_code_begin\n\ts_max_u32 %1, %1, fused_code_end2-fused_code_begin"
                 : "=s"(pc), "=s"(len)::"scc");
    const unsigned off = (idx * 16 + (threadIdx.x & 15)) * 128;
    if (threadIdx.x < 16 && off < len) asm volatile("global_load_dword %0, %1, %2" : "=v"(cv) : "v"(off), "s"(pc) : "memory");
  }
  const rsrc_t rs = __builtin_amdgcn_make_buffer_rsrc((void*)A.ws, 0, WS_END, 0x00020000);
  unsigned epoch;
  unsigned tvw = 0;
  KV kv;
  if (__builtin_expect(cgp < 14, 1)) epoch = phase1_gu(A, rs, lds, rb1, cgp, tvw, kv STAMP_PASS);
  else epoch = phase1_v(A, rs, lds, rb1, cgp, tvw, kv STAMP_PASS);
  PIN(cv);
  phase23(A, rs, lds, rb1, cgp, epoch, tvw, kv STAMP_PASS);
  STAMP_FLUSH(A.ws);
}

extern "C" void kernel_launch(void* const* d_in, const int* in_sizes, int n_in, void* d_out, int out_size, void* d_ws, size_t ws_size,
                              hipStream_t stream) {
  Args A;
  A.x = (const char*)d_in[0]; A.wqkv = (const char*)d_in[1]; A.wo = (const char*)d_in[2];
  A.wg = (const char*)d_in[4]; A.wu = (const char*)d_in[5]; A.wd = (const char*)d_in[6]; A.gw = (const char*)d_in[7];
  A.ws = (char*)d_ws; A.out = (char*)d_out;
  (void)hipFuncSetAttribute((const void*)fused_kernel, hipFuncAttributeMaxDynamicSharedMemorySize, LDS_BYTES);
  hipLaunchKernelGGL(fused_kernel, dim3(256), dim3(512), LDS_BYTES, stream, A);
}
```

```cpp
#include <hip/hip_runtime.h>
#include <hip/hip_bf16.h>
#include <stdint.h>

typedef __attribute__((ext_vector_type(8))) short bf16x8;
typedef __attribute__((ext_vector_type(4))) short s16x4;
typedef __attribute__((ext_vector_type(16))) float f32x16;
typedef __attribute__((ext_vector_type(4))) float f32x4;
typedef __attribute__((ext_vector_type(4))) unsigned u32x4;
typedef __attribute__((ext_vector_type(2))) unsigned u32x2;
typedef __attribute__((ext_vector_type(2))) float f32x2;
typedef __attribute__((ext_vector_type(2))) __bf16 bf16x2_t;
#define LAS __attribute__((address_space(3)))
#define GAS __attribute__((address_space(1)))
typedef LAS const char* lds_cptr;
typedef LAS char* lds_ptr;
typedef __amdgpu_buffer_rsrc_t rsrc_t;

#define DI __device__ __forceinline__
#define MFMA32(a, b, c) __builtin_amdgcn_mfma_f32_32x32x16_bf16((a), (b), (c), 0, 0, 0)
#define SBAR() __builtin_amdgcn_sched_barrier(0)
#define PIN(x) asm volatile("" : "+v"(x))
#define EX2(x) __builtin_amdgcn_exp2f(x)

constexpr int D_ = 448, F_ = 1792;
constexpr int XROW = D_ * 2, QROW = 3 * D_ * 2, FROW = F_ * 2;
constexpr float QSCALE = 0.18898223650461362f * 1.4426950408889634f;
constexpr float LOG2E = 1.4426950408889634f;

constexpr unsigned WS_Q = 0;
constexpr unsigned WS_KV = WS_Q + (2u << 20);
constexpr unsigned WS_H = WS_KV + (4u << 20);
constexpr unsigned WS_O = WS_H + (7u << 20);
constexpr unsigned WS_G = WS_O + (2u << 20);
constexpr unsigned WS_F = WS_G + 8192;
constexpr unsigned F_GEN = WS_F, F_H = WS_F + 4096, F_O = WS_F + 8192, F_QKV = WS_F + 16384;
#define NREP 32
constexpr unsigned WS_END = WS_F + 16384 + 65536;

template <int I> struct IC { static constexpr int v = I; };
template <int B, int E, class Fn> DI void sfor(Fn&& f) {
  if constexpr (B < E) { f(IC<B>{}); sfor<B + 1, E>(f); }
}

DI unsigned pk2(float lo, float hi) {
  f32x2 v = {lo, hi};
  bf16x2_t r = __builtin_convertvector(v, bf16x2_t);
  return __builtin_bit_cast(unsigned, r);
}
DI float rbf(float x) { return __uint_as_float(pk2(x, 0.f) << 16); }
DI u32x4 pack8(const float* v) { return u32x4{pk2(v[0], v[1]), pk2(v[2], v[3]), pk2(v[4], v[5]), pk2(v[6], v[7])}; }

DI void glds16(const char* base, unsigned voff, unsigned lds_base) {
  asm volatile("s_mov_b32 m0, %2\n\ts_nop 0\n\tglobal_load_lds_dwordx4 %0, %1" ::"v"(voff), "s"(base), "s"(lds_base) : "memory");
}
DI void glds16_dev(const char* base, unsigned voff, unsigned lds_base) {
  asm volatile("s_mov_b32 m0, %2\n\ts_nop 0\n\tglobal_load_lds_dwordx4 %0, %1 sc1" ::"v"(voff), "s"(base), "s"(lds_base) : "memory");
}
typedef short v4i16_t __attribute__((ext_vector_type(4)));
DI s16x4 trread(lds_cptr p) {
  return __builtin_bit_cast(s16x4, __builtin_amdgcn_ds_read_tr16_b64_v4i16((LAS v4i16_t*)p));
}
DI bf16x8 ldsread16(lds_cptr p) { return *(const LAS bf16x8*)p; }
DI bf16x8 cat8(s16x4 a, s16x4 b) { return __builtin_shufflevector(a, b, 0, 1, 2, 3, 4, 5, 6, 7); }
template <int N> DI void waitv_bar() {
  SBAR();
  asm volatile("s_waitcnt vmcnt(%0) lgkmcnt(0)\n\ts_barrier" ::"i"(N) : "memory");
  SBAR();
}
template <int N> DI void waitv_bar_nl() {
  SBAR();
  asm volatile("s_waitcnt vmcnt(%0)\n\ts_barrier" ::"i"(N) : "memory");
  SBAR();
}
DI void st16(rsrc_t r, unsigned off, u32x4 v) { __builtin_amdgcn_raw_buffer_store_b128(v, r, off, 0, 16); }
DI void st4(rsrc_t r, unsigned off, unsigned v) { __builtin_amdgcn_raw_buffer_store_b32(v, r, off, 0, 16); }
DI u32x4 ld16(rsrc_t r, unsigned off) { return __builtin_amdgcn_raw_buffer_load_b128(r, off, 0, 16); }
DI unsigned ld4(rsrc_t r, unsigned off) { return __builtin_amdgcn_raw_buffer_load_b32(r, off, 0, 16); }
DI bf16x8 ldfrag(rsrc_t r, unsigned off) { return __builtin_bit_cast(bf16x8, ld16(r, off)); }

struct Args {
  const char *x, *wqkv, *wo, *wg, *wu, *wd, *gw;
  char* ws;
  char* out;
};

constexpr int LDS_BYTES = 163840;
constexpr int SLOT = 40960;
DI constexpr int pos1(int s) { return (s % 3) * SLOT; }

#define QSTEP 30
constexpr int QKV_STEP = QSTEP, O_STEP = 42;
constexpr int SPIN_CAP = 100000;
DI void wait_flags(rsrc_t rs, unsigned off, int n, unsigned epoch, int lane) {
  const unsigned o = off + 4 * (lane < n ? lane : 0);
  for (int spin = 0; spin < SPIN_CAP; ++spin) {
    const unsigned v = ld4(rs, o);
    if (__all(v == epoch)) break;
    __builtin_amdgcn_s_sleep(4);
  }
  asm volatile("" ::: "memory");
}
DI void wait_flags256(rsrc_t rs, unsigned off, unsigned epoch, int lane) {
  bool ok = lane >= 56;
  for (int spin = 0; spin < SPIN_CAP; ++spin) {
    const u32x4 v = ld16(rs, off + 16 * (ok ? 55 : lane));
    ok = ok || (v[0] == epoch && v[1] == epoch && v[2] == epoch && v[3] == epoch);
    if (__all(ok)) break;
    __builtin_amdgcn_s_sleep(8);
  }
  asm volatile("" ::: "memory");
}

DI unsigned look16(const char* p, int wid, int lane, int mask = 15) {
  unsigned v = 0;
  asm volatile("s_cmp_eq_u32 %3, 0\n\ts_cselect_b64 exec, -1, 0\n\tglobal_load_dword %0, %1, %2 sc1\n\ts_mov_b64 exec, -1"
               : "+v"(v) : "v"(4 * (lane & mask)), "s"(p), "s"(wid) : "memory", "scc");
  return v;
}
DI float sigm_mul(float g, float u) { return u * __builtin_amdgcn_rcpf(1.f + EX2(-LOG2E * g)); }

constexpr int RING0 = 114688, KRING = RING0, VRING = RING0 + 24576, VX = 90112, VLAG = 6;
struct KV { const char* kvb; const char* pre; unsigned dst; int lt0; };
DI KV kv_setup(const char* ws, unsigned lds0, int wid) {
  const int bid = blockIdx.x, idx = bid >> 3, hd = 2 * (bid & 7) + (idx & 1), rb = idx >> 1;
  KV k;
  k.kvb = ws + WS_KV + hd * (64 * 4096) + (wid >> 2) * (32 * 4096) + (wid & 1) * 1024;
  k.lt0 = ((wid >> 1) & 1) + 2 * rb;
  k.dst = lds0 + wid * 1024;
  k.pre = ws + (wid < 4 ? WS_KV + hd * (64 * 4096) + (wid >> 1) * (32 * 4096) + (((wid & 1) + 2 * rb) & 31) * 4096 : WS_Q + (hd * 64 + rb * 4 + (wid - 4)) * 2048);
  asm volatile("" : "+s"(k.kvb), "+s"(k.pre), "+s"(k.dst), "+s"(k.lt0));
  return k;
}
DI constexpr int vpos(int u) { return u < 3 ? VX + u * 8192 : VRING + (u % 3) * 8192; }
DI void k_unit(const KV& k, unsigned l16, int s) { glds16_dev(k.kvb + (((2 * s + 2 + k.lt0) & 31) << 12), l16, k.dst + KRING + (s % 3) * 8192); }
DI void v_unit(const KV& k, unsigned l16, int u) { glds16_dev(k.kvb + (((2 * u + k.lt0) & 31) << 12) + 2048, l16, k.dst + vpos(u)); }
DI void p2_first(const KV& k, unsigned lds0, int wid, unsigned l16, int j) {
  if (j >= 2) k_unit(k, l16, j - 2);
  else glds16_dev(k.pre + j * 1024, l16, lds0 + VRING + wid * 2048 + j * 1024);
}
DI void set_qkv_flag(rsrc_t rs, unsigned epoch) {
  if (threadIdx.x < NREP) st4(rs, F_QKV + 1024 * threadIdx.x + 4 * blockIdx.x, epoch);
}

DI unsigned touch(const Args& A, int wid, int lane, int r) {
  const int w = blockIdx.x * 8 + wid;
  unsigned v;
  if (r == 0) {
    const int row = (w - 1792) * 2 + ((lane >> 3) & 1), l = lane & 7;
    asm volatile("global_load_dword %0, %1, %2" : "=v"(v) : "v"((row < 448 ? row : 447) * QROW + (14 + (l < 7 ? l : 6)) * 128), "s"(A.wqkv) : "memory");
  } else {
    const int lo = 2 * w + (lane & 1), l7 = lane & 7;
    const char* p = (lane & 8) ? A.wo + (lo < 3136 ? lo : 3135) * 128 : A.wd + (7 * w + (l7 < 7 ? l7 : 6)) * 128;
    asm volatile("global_load_dword %0, %1, off" : "=v"(v) : "v"(p) : "memory");
  }
  return v;
}
DI unsigned load_gen(const Args& A) {
  unsigned v;
  asm volatile("global_load_dword %0, %1, %2 sc1" : "=v"(v) : "v"(0), "s"(A.ws + F_GEN) : "memory");
  return v;
}
DI unsigned epoch_of(unsigned genv) { return __builtin_amdgcn_readfirstlane(genv) * 1664525u + 1013904223u; }

DI int krot(int rb, int cgp) { return cgp < 14 ? (cgp + 2 * rb + (rb >> 2)) % 7 : 0; }
DI int kch(int s, int rot) { const int c = s + rot; return c >= 7 ? c - 7 : c; }

DI constexpr int posA(int s) { return s == 0 || s == 6 ? 139264 : 16384 + (s - 1) * 24576; }
DI unsigned phase1_gu(const Args& A, rsrc_t rs, char* lds, int rb, int cgp, unsigned& tvw, KV& kv) {
  const int tid = threadIdx.x, lane = tid & 63, wid = __builtin_amdgcn_readfirstlane(tid >> 6);
  const int hh = lane >> 5, l31 = lane & 31, rsb = wid & 3, cg = wid >> 2;
  const int q4 = (lane & 15) >> 2, p4 = lane & 3, blk = (lane >> 4) & 1;
  const unsigned lds0 = (unsigned)(uintptr_t)lds;
  const lds_cptr L = (lds_cptr)(LAS char*)(uintptr_t)lds0;
  const int r8 = lane >> 3, c8 = lane & 7;
  const int xr0 = 16 * wid + r8;
  const char* xs = A.x + (size_t)(128 * rb + 16 * wid) * XROW;
  const unsigned xc0 = r8 * XROW + 16 * (c8 ^ ((xr0 >> 1) & 7)), xc1 = (r8 + 8) * XROW + 16 * (c8 ^ (((xr0 + 8) >> 1) & 7));
  const int sw0 = 32 * ((c8 >> 1) ^ (r8 & 3)) + 16 * (c8 & 1);
  const unsigned oq = r8 * QROW + sw0, og = r8 * FROW + sw0, ou = og ^ 32;
  const char* wq = A.wqkv + (size_t)(8 * wid) * QROW + 128 * cgp;
  const char* wg = A.wg + (size_t)(8 * wid) * FROW + 256 * cgp;
  const char* wu = A.wu + (size_t)(8 * wid) * FROW + 256 * cgp;
  const int rot = krot(rb, cgp);
  auto pieceA = [&](int s, int p) {
    const unsigned sb = lds0 + posA(s);
    const int c = kch(s, rot);
    if (p == 0) glds16(xs + 128 * c, xc0, sb + 2048 * wid);
    if (p == 1) glds16(xs + 128 * c, xc1, sb + 2048 * wid + 1024);
    if (p == 2) glds16(wq + c * (64 * QROW), oq, sb + 16384 + 1024 * wid);
  };
  auto stageA = [&](int s) { pieceA(s, 0); pieceA(s, 1); pieceA(s, 2); };
  auto pieceU = [&](int u, int c, int p) {
    glds16((p ? wu : wg) + kch(c, rot) * (64 * FROW) + 128 * u, p ? ou : og, lds0 + c * 16384 + wid * 2048 + p * 1024);
  };
  pieceA(0, 2);
  unsigned genv;
  stageA(1); pieceU(0, 0, 0); pieceU(0, 0, 1); stageA(2);

  const int rt32 = rb * 4 + rsb;
  const int xrow = 32 * rsb + l31, xsw = (xrow >> 1) & 7;
  lds_cptr xb[4];
#pragma unroll
  for (int j = 0; j < 4; ++j) xb[j] = L + xrow * 128 + 16 * (((j << 1) | hh) ^ xsw);
  const lds_cptr wt0 = L + 16384 + (8 * hh + q4) * 128 + 8 * p4 + 32 * ((2 * cg + blk) ^ q4);
  const int rowb = hh * 2048 + blk * 1024 + q4 * 128 + 8 * p4;
  const lds_cptr wb0 = L + rowb + 32 * ((2 * cg) ^ q4 ^ blk), wb1 = L + rowb + 32 * ((2 * cg + 1) ^ q4 ^ blk);
  const bool isq = cgp < 7;
  const unsigned hs = isq ? 2048u : 4096u, pad = isq ? 0u : 0x3f80u;
  unsigned qo[2]; bool str[2];
#pragma unroll
  for (int t = 0; t < 2; ++t) {
    const int P = 8 * (isq ? cgp : cgp - 7) + 4 * cg + 2 * t + hh;
    const int hp = (P * 37) >> 8, r = P - 7 * hp;
    const int head = 2 * hp + (r > 3 ? 1 : 0), piece = r > 3 ? r - 4 : r;
    qo[t] = (isq ? WS_Q : WS_KV) + (head * 64 + rt32) * hs + l31 * 16 + piece * 512;
    str[t] = r == 3;
    PIN(qo[t]);
  }
  const float qs = isq ? QSCALE : 1.f;
  unsigned hbase = WS_H + (rt32 * 112 + 8 * cgp + 2 * cg) * 1024 + lane * 16;
  PIN(hbase);
  kv = kv_setup(A.ws, lds0, wid);

  bf16x8 xf[28];
  f32x16 a0, a1, a2, b0, b1, zero16;
#pragma unroll
  for (int i = 0; i < 16; ++i) zero16[i] = 0.f;
  bf16x8 wf[4];
  lds_cptr c0, c1;
  auto rdG = [&](auto Q) {
    constexpr int q = decltype(Q)::v;
    if constexpr (q < 56) {
      constexpr int i = q >> 1;
      if constexpr (q % 28 == 0) { c0 = wb0 + i * 4096; c1 = wb1 + i * 4096; PIN(c0); PIN(c1); }
      const lds_cptr b = (q & 1) ? c1 : c0;
      wf[(q + 2) & 3] = cat8(trread(b + (i % 14) * 4096), trread(b + (i % 14) * 4096 + 512));
    } else if constexpr (q < 112) {
      constexpr int kk = (q - 56) % 28;
      if constexpr (kk % 14 == 0) { c0 = (q < 84 ? wb0 : wb1) + kk * 4096; PIN(c0); }
      wf[(q + 2) & 3] = cat8(trread(c0 + (kk % 14) * 4096), trread(c0 + (kk % 14) * 4096 + 512));
    }
  };

  {
    bf16x8 fq[2][4];
    sfor<0, 8>([&](auto S) {
      constexpr int s = decltype(S)::v;
      if constexpr (s < 7) {
        waitv_bar<(s == 1 ? 9 : s < 3 ? 8 : s + 6)>();
        lds_cptr cq = wt0 + posA(s);
        PIN(cq);
#pragma unroll
        for (int j = 0; j < 4; ++j) {
          xf[4 * s + j] = ldsread16(xb[j] + posA(s));
          fq[s & 1][j] = cat8(trread(cq + j * 2048), trread(cq + j * 2048 + 512));
        }
      } else { rdG(IC<0>{}); rdG(IC<1>{}); rdG(IC<2>{}); }
      SBAR();
      sfor<0, 4>([&](auto J) {
        constexpr int j = decltype(J)::v, i = 4 * (s - 1) + j, n = 4 * s + j;
        if constexpr (s > 0) a0 = MFMA32(fq[(s - 1) & 1][j], xf[i], i == 0 ? zero16 : a0);
        SBAR();
        if constexpr (n < 12) pieceA(3 + n / 3, n % 3);
        else if constexpr (n == 12) genv = load_gen(A);
        else if constexpr (n < 25) pieceU(0, 1 + (n - 13) / 2, (n - 13) & 1);
        SBAR();
      });
    });
  }
  unsigned epoch;
  u32x4 qk[2];
  float hv[8];
  auto poll = [&]() { glds16_dev(A.ws + F_QKV + 1024 * (blockIdx.x & (NREP - 1)), lane * 16, lds0 + VRING + wid * 2048); };
  sfor<0, 112>([&](auto Q) {
    constexpr int q = decltype(Q)::v, i = q - 56;
    if constexpr (q < 64 && (q & 7) == 5) {
      constexpr int c = q >> 3;
      waitv_bar_nl<(c == 0 ? 12 : c == 1 ? 10 : c == 2 ? 10 : c == 3 ? 4 : c == 6 ? 6 : c == 7 ? 4 : 20)>();
      if constexpr (c == 0) { PIN(genv); epoch = epoch_of(genv); }
      if constexpr (c == 3) { set_qkv_flag(rs, epoch); }
    }
    if constexpr (i == 13) { waitv_bar_nl<2>(); }
    if constexpr (i == 21) { waitv_bar_nl<1>(); }
    if constexpr (i == QKV_STEP) {
      if (wid == 0) {
        asm volatile("s_waitcnt vmcnt(0)" ::: "memory");
        const u32x4 f = __builtin_bit_cast(u32x4, ldsread16(L + VRING + lane * 16));
        if (!__all(lane >= 56 || (f[0] == epoch && f[1] == epoch && f[2] == epoch && f[3] == epoch))) wait_flags256(rs, F_QKV + 1024 * (blockIdx.x & (NREP - 1)), epoch, lane);
      }
      waitv_bar_nl<20>();
    }
    rdG(IC<q + 3>{});
    SBAR();
    if constexpr (q < 56) {
      if constexpr (q & 1) a2 = MFMA32(wf[(q + 2) & 3], xf[q >> 1], q < 2 ? zero16 : a2);
      else a1 = MFMA32(wf[(q + 2) & 3], xf[q >> 1], q < 2 ? zero16 : a1);
    } else if constexpr (i < 28) b0 = MFMA32(wf[(q + 2) & 3], xf[i], i == 0 ? zero16 : b0);
    else b1 = MFMA32(wf[(q + 2) & 3], xf[i - 28], i == 28 ? zero16 : b1);
    SBAR();
    if constexpr (q >= 13 && q < 64 && ((q & 7) == 5 || (q & 7) == 6)) pieceU(1, (q >> 3) - 1, (q & 7) - 5);
    if constexpr (q == 0 || q == 2) {
      constexpr int t = q >> 1;
      const unsigned w0 = pk2(a0[8 * t] * qs, a0[8 * t + 1] * qs), w1 = pk2(a0[8 * t + 2] * qs, a0[8 * t + 3] * qs);
      const unsigned w2 = pk2(a0[8 * t + 4] * qs, a0[8 * t + 5] * qs), w3 = pk2(a0[8 * t + 6] * qs, a0[8 * t + 7] * qs);
      auto r0 = __builtin_amdgcn_permlane32_swap(w0, w2, false, false);
      auto r1 = __builtin_amdgcn_permlane32_swap(w1, w3, false, false);
      qk[t] = u32x4{r0[0], r1[0], r0[1], r1[1]};
      PIN(qk[t]);
    }
    if constexpr (q == 1 || q == 3) {
      constexpr int t = q >> 1;
      u32x4 v = qk[t];
      if (str[t]) { v[2] = pad; v[3] = 0u; }
      st16(rs, qo[t], v);
      if (str[t]) st16(rs, qo[t] + 64 * hs, u32x4{qk[t][2], qk[t][3], pad, 0u});
    }
    if constexpr (i >= 1 && i < 52 && (i - 1) % 17 < 16) {
      constexpr int t = (i - 1) / 17, e = ((i - 1) % 17) >> 1;
      const f32x16& a = t == 0 ? a1 : t == 1 ? a2 : b0;
      if constexpr (((i - 1) % 17) & 1) hv[e] = a[e + 8] * __builtin_amdgcn_rcpf(1.f + hv[e]);
      else hv[e] = EX2(-LOG2E * a[e]);
      PIN(hv[e]);
    }
    if constexpr (i == 17) st16(rs, hbase, pack8(hv));
    if constexpr (i == 34) st16(rs, hbase + 1024, pack8(hv));
    if constexpr (i == 51) st16(rs, hbase + 4096, pack8(hv));
    if constexpr (i == 35) tvw = touch(A, wid, lane, 4);
    if constexpr (i == 0) { if (wid == 0) poll(); }
    if constexpr (i >= QKV_STEP && i < QKV_STEP + 4) p2_first(kv, lds0, wid, lane * 16, i - QKV_STEP);
    SBAR();
  });
#pragma unroll
  for (int i = 0; i < 8; ++i) hv[i] = sigm_mul(b1[i], b1[i + 8]);
  st16(rs, hbase + 5120, pack8(hv));
  asm volatile("s_waitcnt vmcnt(4)" ::: "memory");
  return epoch;
}

DI unsigned phase1_v(const Args& A, rsrc_t rs, char* lds, int rb, int cgp, unsigned& tvw, KV& kv) {
  const int tid = threadIdx.x, lane = tid & 63, wid = __builtin_amdgcn_readfirstlane(tid >> 6);
  const int hh = lane >> 5, l31 = lane & 31, rsb = wid & 3, cg = wid >> 2;
  const int q4 = (lane & 15) >> 2, p4 = lane & 3, blk = (lane >> 4) & 1;
  const unsigned lds0 = (unsigned)(uintptr_t)lds;
  const lds_cptr L = (lds_cptr)(LAS char*)(uintptr_t)lds0;
  const int r8 = lane >> 3, c8 = lane & 7;
  const int xr0 = 16 * wid + r8;
  const char* xs = A.x + (size_t)(128 * rb + 16 * wid) * XROW;
  const unsigned xc0 = r8 * XROW + 16 * (c8 ^ ((xr0 >> 1) & 7)), xc1 = (r8 + 8) * XROW + 16 * (c8 ^ (((xr0 + 8) >> 1) & 7));
  const unsigned oq = r8 * QROW + 32 * ((c8 >> 1) ^ (r8 & 3)) + 16 * (c8 & 1);
  const int l0 = cgp == 14 ? 14 : 18;
  const char* wa = A.wqkv + (size_t)(8 * wid) * QROW + 128 * l0;
  const char* wbs = A.wqkv + 128 * 17 + 64 * (cgp - 14);
  auto piece = [&](int s, int p) {
    if (s < 7) {
      const unsigned sb = lds0 + pos1(s);
      if (p == 0) glds16(xs + 128 * s, xc0, sb + 2048 * wid);
      if (p == 1) glds16(xs + 128 * s, xc1, sb + 2048 * wid + 1024);
      if (p >= 2) glds16(wa + (size_t)(64 * s) * QROW + 128 * (p - 2), oq, sb + 16384 + 8192 * (p - 2) + 1024 * wid);
    }
  };
  auto piece17 = [&](int p) {
    const int g = wid < 7 ? 4 * wid + p : 24 + p;
    glds16(wbs + (size_t)(16 * g) * QROW, (lane >> 2) * QROW + (lane & 3) * 16, lds0 + 122880 + g * 1024);
  };
#pragma unroll
  for (int p = 2; p < 5; ++p) piece(0, p);
  unsigned tv0 = touch(A, wid, lane, 0), genv;
#pragma unroll
  for (int p = 0; p < 5; ++p) piece(1, p);
  const int rt32 = rb * 4 + rsb;
  const int xrow = 32 * rsb + l31, xsw = (xrow >> 1) & 7;
  lds_cptr xb[4];
#pragma unroll
  for (int j = 0; j < 4; ++j) xb[j] = L + xrow * 128 + 16 * (((j << 1) | hh) ^ xsw);
  const lds_cptr wt0 = L + 16384 + (8 * hh + q4) * 128 + 8 * p4 + 32 * ((2 * cg + blk) ^ q4);
  lds_cptr wb0 = L + 122880 + (8 * hh + q4) * 64 + 32 * blk + 8 * p4;
  PIN(wb0);
  unsigned vo[4];
#pragma unroll
  for (int t = 0; t < 4; ++t) {
    const int tile = t < 3 ? 2 * l0 + 2 * t + cg : 34 + cgp - 14;
    const int n = 32 * (tile - 28) + l31, head = (n * 2341) >> 16, dh = n - 28 * head;
    vo[t] = WS_KV + ((head * 64 + rt32) * 4 + 2) * 1024 + (dh + 32 * hh) * 16;
    PIN(vo[t]);
  }
  kv = kv_setup(A.ws, lds0, wid);
  bf16x8 xf[28];
  f32x16 a0, a1, a2, b0, zero16;
#pragma unroll
  for (int i = 0; i < 16; ++i) zero16[i] = 0.f;
  bf16x8 wf[6];
  lds_cptr cx[4], c0;
  auto rdA = [&](auto I, auto M) {
    constexpr int i = decltype(I)::v, m = decltype(M)::v, s = i >> 2, j = i & 3, w = (i & 1) * 3;
    if constexpr (m == 0 && j == 0) {
#pragma unroll
      for (int t = 0; t < 4; ++t) { cx[t] = xb[t] + pos1(s); PIN(cx[t]); }
      c0 = wt0 + pos1(s); PIN(c0);
    }
    if constexpr (m == 0) xf[i] = ldsread16(cx[j]);
    wf[w + m] = cat8(trread(c0 + m * 8192 + j * 2048), trread(c0 + m * 8192 + j * 2048 + 512));
  };
  auto rdB = [&](auto I) {
    constexpr int i = decltype(I)::v;
    wf[i & 3] = cat8(trread(wb0 + i * 1024), trread(wb0 + i * 1024 + 256));
  };
  waitv_bar<6>();
  rdA(IC<0>{}, IC<0>{}); rdA(IC<0>{}, IC<1>{}); rdA(IC<0>{}, IC<2>{});
  sfor<0, 28>([&](auto I) {
    constexpr int i = decltype(I)::v, s = i >> 2, w = (i & 1) * 3;
    auto vm = [&](auto M) {
      constexpr int m = decltype(M)::v, r = i & 3;
      if constexpr (m == 0) piece(s + 2, r == 0 ? 0 : r + 1);
      if constexpr (m == 1 && r == 0) piece(s + 2, 1);
      if constexpr (i == 11 && m == 1) genv = load_gen(A);
      if constexpr (i >= 20 && i < 22 && m < 2) piece17(2 * (i - 20) + m);
      if constexpr (i == 22 && m == 0) glds16(A.gw, (lane < 56 ? lane : 55) * 16, lds0 + 151552 + 1024 * wid);
    };
    auto rd = [&](auto M) { if constexpr (i < 27) rdA(IC<i + 1>{}, M); else rdB(M); };
    SBAR();
    a0 = MFMA32(xf[i], wf[w], i == 0 ? zero16 : a0);
    if constexpr ((i & 3) == 3) {
      waitv_bar<(s == 3 ? 5 : s == 5 ? 5 : s == 6 ? 0 : 4)>();
      if constexpr (s == 0) PIN(tv0);
      if constexpr (s == 4) PIN(genv);
    }
    SBAR(); rd(IC<0>{}); vm(IC<0>{}); SBAR();
    a1 = MFMA32(xf[i], wf[w + 1], i == 0 ? zero16 : a1);
    SBAR(); rd(IC<1>{}); vm(IC<1>{}); SBAR();
    a2 = MFMA32(xf[i], wf[w + 2], i == 0 ? zero16 : a2);
    SBAR(); rd(IC<2>{}); vm(IC<2>{}); SBAR();
  });
  const unsigned epoch = epoch_of(genv);
  auto vstore = [&](const f32x16& a, unsigned o) {
    float v[16];
#pragma unroll
    for (int i = 0; i < 16; ++i) v[i] = a[i];
    st16(rs, o, pack8(v)); st16(rs, o + 1024, pack8(v + 8));
  };
  {
    const int head = 8 * (cgp - 14) + 4 * cg + (lane >> 4);
    const unsigned o = WS_KV + ((head * 64 + rt32) * 4 + 2 + ((lane >> 3) & 1)) * 1024 + (28 + (lane & 3) + 32 * ((lane >> 2) & 1)) * 16;
    const unsigned v = (lane & 3) == 0 ? 0x3f803f80u : 0u;
    st16(rs, o, u32x4{v, v, v, v});
  }
  if (cg == 0) {
    sfor<0, 28>([&](auto I) {
      constexpr int i = decltype(I)::v;
      if constexpr (i + 3 < 28) rdB(IC<i + 3>{});
      SBAR();
      b0 = MFMA32(xf[i], wf[i & 3], i == 0 ? zero16 : b0);
      if constexpr (i == 2) vstore(a0, vo[0]);
      if constexpr (i == 5) vstore(a1, vo[1]);
      if constexpr (i == 9) vstore(a2, vo[2]);
      SBAR();
    });
    vstore(b0, vo[3]);
  } else {
    vstore(a0, vo[0]); vstore(a1, vo[1]); vstore(a2, vo[2]);
    if (wid == 4) wait_flags256(rs, F_QKV + 1024 * (blockIdx.x & (NREP - 1)), epoch, lane);
  }
  waitv_bar<0>();
  set_qkv_flag(rs, epoch);
#pragma unroll
  for (int j = 0; j < 4; ++j) p2_first(kv, lds0, wid, lane * 16, j);
  if (cg == 1 && cgp == 14) {
    const lds_cptr gp = L + 151552 + 1024 * wid + 16 * hh;
    f32x16 ga = zero16;
#pragma unroll
    for (int i = 0; i < 28; ++i) ga = MFMA32(ldsread16(gp + 32 * i), xf[i], ga);
    asm volatile("s_waitcnt vmcnt(0)" ::: "memory");
    const float g = rbf(1.f / (1.f + __expf(-rbf(ga[0]))));
    if (lane < 32) st4(rs, WS_G + 4 * (32 * rt32 + lane), __float_as_uint(g));
  } else asm volatile("s_waitcnt vmcnt(0)" ::: "memory");
  asm volatile("fused_code_end2:" ::: "memory");
  return epoch;
}

DI float xhalf_max(float v) {
  auto r = __builtin_amdgcn_permlane32_swap(__float_as_uint(v), __float_as_uint(v), false, false);
  return fmaxf(__uint_as_float(r[0]), __uint_as_float(r[1]));
}
DI float max16(const f32x16& s) {
  float a = fmaxf(fmaxf(s[0], s[1]), s[2]), b = fmaxf(fmaxf(s[3], s[4]), s[5]);
  a = fmaxf(fmaxf(a, s[6]), s[7]); b = fmaxf(fmaxf(b, s[8]), s[9]);
  a = fmaxf(fmaxf(a, s[10]), s[11]); b = fmaxf(fmaxf(b, s[12]), s[13]);
  a = fmaxf(fmaxf(a, s[14]), s[15]);
  return fmaxf(a, b);
}

DI void phase23(const Args& A, rsrc_t rs, char* lds, int rb1, int cgp, unsigned epoch, unsigned tvw, const KV& kv) {
  const int bid = blockIdx.x, xcd = bid & 7, idx = bid >> 3;
  const int hd = 2 * xcd + (idx & 1), rb = idx >> 1;
  const int i3 = idx < 28 ? idx : idx - 28, rt64 = 4 * xcd + i3 / 7, c7 = i3 % 7;
  const int tid = threadIdx.x, lane = tid & 63, wid = __builtin_amdgcn_readfirstlane(tid >> 6);
  const int hh = lane >> 5, qs = wid & 3, kh = wid >> 2;
  const unsigned lds0 = (unsigned)(uintptr_t)lds;
  const lds_cptr L = (lds_cptr)(LAS char*)(uintptr_t)lds0;
  const unsigned kvo = WS_KV + hd * (64 * 4096) + kh * (32 * 4096);
  const unsigned l16 = lane * 16;
  const int r3 = wid & 1, kq = wid >> 1;
  const int r8 = lane >> 3, c8 = lane & 7;
  const unsigned ow = r8 * XROW + 32 * ((c8 >> 1) ^ (r8 & 3)) + 16 * (c8 & 1);
  const char* wds = A.wd + (size_t)(448 * kq + 16 * r3) * XROW + 128 * c7;
  auto stage_wd = [&](int c, int slot, int p) {
    glds16(wds + (size_t)(32 * c + 8 * p) * XROW, ow, lds0 + slot * 16384 + wid * 2048 + p * 1024);
  };

  waitv_bar<20>();
  const unsigned fvo = F_QKV + 1024 * (bid & (NREP - 1)) + 896;
  unsigned fv = look16(A.ws + fvo, wid, lane, 31);
  const lds_cptr pb = L + VRING + l16;
  const bf16x8 q0 = ldsread16(pb + 8192 + qs * 2048), q1 = ldsread16(pb + 8192 + qs * 2048 + 1024);
  const bf16x8 ka0 = ldsread16(pb + kh * 4096), ka1 = ldsread16(pb + kh * 4096 + 1024);
  bf16x8 kf0 = ldsread16(pb + kh * 4096 + 2048), kf1 = ldsread16(pb + kh * 4096 + 3072);
  f32x16 zero16;
#pragma unroll
  for (int i = 0; i < 16; ++i) zero16[i] = 0.f;
  f32x16 sA = MFMA32(ka0, q0, zero16);
  float mref;
  {
    f32x16 s = MFMA32(ka1, q1, sA);
    mref = rbf(xhalf_max(max16(s)));
  }
  bf16x8 q1m = q1;
  if (hh) q1m[4] = (short)((__float_as_uint(-mref)) >> 16);
  sA = MFMA32(ka1, q1m, sA);
  PIN(kf0); PIN(kf1);
  f32x16 o = zero16, sB;
  bf16x8 vf0, vf1, xf0, xf1;
  u32x4 P0[VLAG + 1], P1[VLAG + 1];
  float e0 = 0.f, e1 = 0.f, e2 = 0.f, e3 = 0.f;
  lds_cptr rk = L + KRING + kh * 4096 + l16, rx = L + VX + kh * 4096 + l16;
  PIN(rk); PIN(rx);
  bf16x8 hf[28];
  unsigned fh;
  float g;
  const unsigned ho = WS_H + ((rt64 * 2 + r3) * 112 + 28 * kq) * 1024 + lane * 16;
#define B8(x) __builtin_bit_cast(bf16x8, x)
#define G(MF, RDS, W0, W1, C, i) do { MF; SBAR(); RDS; SBAR(); \
    float n0 = EX2(C[i]); PIN(n0); SBAR(); W0 = pk2(e0, e1); PIN(W0); SBAR(); float n1 = EX2(C[i + 1]); PIN(n1); SBAR(); W1 = pk2(e2, e3); PIN(W1); SBAR(); \
    float n2 = EX2(C[i + 2]); PIN(n2); float n3 = EX2(C[i + 3]); PIN(n3); e0 = n0; e1 = n1; e2 = n2; e3 = n3; SBAR(); } while (0)
  sfor<0, 16>([&](auto S) {
    constexpr int s = decltype(S)::v;
    constexpr auto D = [](int t) { return t < 0 ? 0 : (t + 2 <= 14 ? 1 : 0) + (t >= 1 && t + 2 <= 15 ? 1 : 0) + (t >= 1 && t <= 3 ? 1 : 0); };
    constexpr auto E = [](int t) { return t < 0 ? 0 : (t < 5 || t == 6 || t == 7) ? 2 : (t >= 8 && t < 15) ? 4 : 0; };
    constexpr int N = s == 0 ? 20 : E(s - 2) + D(s - 1) + E(s - 1);
    if constexpr (s == 1) {
      asm volatile("s_waitcnt vmcnt(3)" : "+v"(fv)::"memory");
      if (wid == 0 && !__all(fv == epoch)) wait_flags(rs, fvo, 32, epoch, lane);
    }
    if constexpr (s == 8) {
      PIN(fh);
      if (wid == 0 && !__all(fh == epoch)) wait_flags(rs, F_H + 128 * (rt64 >> 1), 16, epoch, lane);
    }
    waitv_bar<N>();
    if constexpr (s == 1) { PIN(tvw); }
    if constexpr (s == 1) { if (tid == 0) st4(rs, F_H + 128 * rb1 + 4 * cgp, epoch); }
    if constexpr (s == 5) fh = look16(A.ws + F_H + 128 * (rt64 >> 1), wid, lane);
    auto vm = [&](auto J) {
      constexpr int j = decltype(J)::v, d = D(s), e = j - d;
      if constexpr (j < d) {
        if constexpr (j == 0 && s + 2 <= 14) k_unit(kv, l16, s + 2);
        else if constexpr (j == 2) v_unit(kv, l16, s - 1);
        else v_unit(kv, l16, s + 2);
      } else if constexpr (e < E(s)) {
        if constexpr (s < 8) stage_wd(s < 5 ? s : s - 1, s < 5 ? s : s - 1, e);
        else hf[4 * (s - 8) + e] = ldfrag(rs, ho + (4 * (s - 8) + e) * 1024);
      } else if constexpr (s == 15 && j == 2) g = __uint_as_float(ld4(rs, WS_G + 4 * (rt64 * 64 + r3 * 32 + (lane & 31))));
    };
    SBAR();
    sfor<0, 2>([&](auto T) {
      constexpr int t = decltype(T)::v, i = 2 * s + t;
      constexpr int pc = i < VLAG ? i : VLAG, pp = i - 1 < VLAG ? (i > 0 ? i - 1 : VLAG) : VLAG;
      constexpr bool pv = i >= VLAG, cu = i >= VLAG && i < 2 * VLAG;
      constexpr int ko = (s % 3) * 8192 + t * 2048, vo = 24576 + ko, xo = ((s - 3) % 3) * 8192 + t * 2048;
      f32x16& C = t ? sB : sA;
      f32x16& Nx = t ? sA : sB;
      __builtin_amdgcn_s_setprio(3 - 2 * t);
      G(if constexpr (i < 31) Nx = MFMA32(kf0, q0, zero16),
        if constexpr (cu) { xf0 = ldsread16(rx + xo); xf1 = ldsread16(rx + xo + 1024); } vm(IC<3 * t>{}), P1[pp][2], P1[pp][3], C, 0);
      G(if constexpr (i > VLAG) o = MFMA32(vf1, B8(P1[VLAG]), o),
        if constexpr (pv) vf0 = ldsread16(rk + vo); vm(IC<3 * t + 1>{}), P0[pc][0], P0[pc][1], C, 4);
      if constexpr (cu) { o = MFMA32(xf0, B8(P0[i - VLAG]), o); SBAR(); }
      __builtin_amdgcn_s_setprio(2 - 2 * t);
      G(if constexpr (i < 31) Nx = MFMA32(kf1, q1m, Nx),
        if constexpr (pv) vf1 = ldsread16(rk + vo + 1024); if constexpr (i < 30) { kf0 = ldsread16(rk + ko); kf1 = ldsread16(rk + ko + 1024); }, P0[pc][2], P0[pc][3], C, 8);
      G(if constexpr (pv) o = MFMA32(vf0, B8(P0[VLAG]), o), vm(IC<3 * t + 2>{}), P1[pc][0], P1[pc][1], C, 12);
      if constexpr (cu) { o = MFMA32(xf1, B8(P1[i - VLAG]), o); SBAR(); }
    });
  });
  P1[VLAG][2] = pk2(e0, e1); P1[VLAG][3] = pk2(e2, e3);
  o = MFMA32(vf1, B8(P1[VLAG]), o);
#undef G
  {
    float t0 = fabsf(o[0]) + fabsf(o[1]), t1 = fabsf(o[2]) + fabsf(o[3]);
#pragma unroll
    for (int i = 4; i < 16; i += 4) { t0 += fabsf(o[i]) + fabsf(o[i + 1]); t1 += fabsf(o[i + 2]) + fabsf(o[i + 3]); }
    const bool bad = !(t0 + t1 <= 3.0e38f) || (hh && !(o[12] >= 0x1p-20f && o[12] <= 0x1p80f));
    if (__builtin_expect(__any(bad), 0)) {
      float m = -INFINITY;
      o = zero16;
#pragma unroll 1
      for (int t = 0; t < 32; ++t) {
        const unsigned p = kvo + t * 4096 + lane * 16;
        f32x16 s = MFMA32(ldfrag(rs, p), q0, zero16);
        s = MFMA32(ldfrag(rs, p + 1024), q1, s);
        const float mn = fmaxf(m, xhalf_max(max16(s)));
        const float al = EX2(m - mn);
        m = mn;
        float e[16];
#pragma unroll
        for (int i = 0; i < 16; ++i) { o[i] *= al; e[i] = EX2(s[i] - mn); }
        o = MFMA32(ldfrag(rs, p + 2048), __builtin_bit_cast(bf16x8, pack8(e)), o);
        o = MFMA32(ldfrag(rs, p + 3072), __builtin_bit_cast(bf16x8, pack8(e + 8)), o);
      }
      mref = m;
    }
  }
  {
    auto lr = __builtin_amdgcn_permlane32_swap(__float_as_uint(o[12]), __float_as_uint(o[12]), false, false);
    const float lown = __uint_as_float(lr[1]);
    f32x4* xs = (f32x4*)(lds + KRING) + wid * (3 * 64) + lane;
    const f32x4* xr = (const f32x4*)(lds + KRING) + (wid ^ 4) * (3 * 64) + lane;
    f32x4 k0, k1;
    if (kh == 0) { xs[0] = f32x4{o[8], o[9], o[10], o[11]}; xs[64] = f32x4{o[12], o[13], o[14], o[15]}; k0 = f32x4{o[0], o[1], o[2], o[3]}; k1 = f32x4{o[4], o[5], o[6], o[7]}; }
    else { xs[0] = f32x4{o[0], o[1], o[2], o[3]}; xs[64] = f32x4{o[4], o[5], o[6], o[7]}; k0 = f32x4{o[8], o[9], o[10], o[11]}; k1 = f32x4{o[12], o[13], o[14], o[15]}; }
    xs[128] = f32x4{mref, lown, 0.f, 0.f};
    waitv_bar<0>();
    PIN(g);
    const f32x4 t0 = xr[0], t1 = xr[64], ml = xr[128];
    const float M = fmaxf(mref, ml[0]);
    float a0 = EX2(mref - M), a1 = EX2(ml[0] - M);
    const float inv = 1.f / (lown * a0 + ml[1] * a1);
    a0 *= inv; a1 *= inv;
    float v[8];
#pragma unroll
    for (int j = 0; j < 4; ++j) { v[j] = k0[j] * a0 + t0[j] * a1; v[4 + j] = k1[j] * a0 + t1[j] * a1; }
    if (kh && hh) { v[4] = 0.f; v[5] = 0.f; v[6] = 0.f; v[7] = 0.f; }
    st16(rs, WS_O + (((rb * 4 + qs) * 16 + hd) * 2 + kh) * 1024 + l16, pack8(v));
  }
  if (idx >= 28) {
    waitv_bar<0>();
    if (tid == 0) st4(rs, F_O + 128 * rb + 4 * hd, epoch);
    if (bid == 255 && tid == 0) st4(rs, F_GEN, epoch);
    return;
  }

  const int q4 = (lane & 15) >> 2, p4 = lane & 3, blk = (lane >> 4) & 1;
  const int wro = (4 * hh + q4) * 128 + 8 * p4;
  const lds_cptr wd0 = L + kq * 4096 + wro + 32 * ((0 + blk) ^ q4), wd1 = L + kq * 4096 + wro + 32 * ((2 + blk) ^ q4);
  f32x16 bc0, bc1;
  bf16x8 wf[4], of[8];
  lds_cptr d0, d1;
  auto rdW = [&](auto Q) {
    constexpr int q = decltype(Q)::v, sl = (q >> 2) % 10, so = (sl % 3) * 16384 + ((q >> 1) & 1) * 2048;
    if constexpr (q < 56) {
      if constexpr ((q & 3) == 0 && sl % 3 == 0) { d0 = wd0 + sl * 16384; d1 = wd1 + sl * 16384; PIN(d0); PIN(d1); }
      const lds_cptr b = (q & 1) ? d1 : d0;
      wf[q & 3] = cat8(trread(b + so), trread(b + so + 1024));
    }
  };
  const char* wos = A.wo + (size_t)(56 * wid) * XROW + 128 * c7;
  const unsigned oo = WS_O + (((rt64 * 2 + r3) * 16 + 4 * kq) * 2) * 1024 + l16;
  unsigned fo;
  rdW(IC<0>{}); rdW(IC<1>{}); rdW(IC<2>{});
  sfor<0, 56>([&](auto Q) {
    constexpr int q = decltype(Q)::v;
    if constexpr (q == 2) waitv_bar_nl<20>();
    if constexpr (q == 16) {
      waitv_bar_nl<6>();
      if (tid == 0) st4(rs, F_O + 128 * rb + 4 * hd, epoch);
    }
    if constexpr (q == 25) waitv_bar_nl<8>();
    if constexpr (q == 34) waitv_bar_nl<20>();
    if constexpr (q == 37) waitv_bar_nl<4>();
    if constexpr (q == O_STEP) {
      asm volatile("s_waitcnt vmcnt(7)" : "+v"(fo)::"memory");
      if (wid == 0 && !__all(fo == epoch)) wait_flags(rs, F_O + 128 * (rt64 >> 1), 16, epoch, lane);
      waitv_bar_nl<20>();
    }
    rdW(IC<q + 3>{});
    SBAR();
    if constexpr (q & 1) bc1 = MFMA32(wf[q & 3], hf[q >> 1], q < 2 ? zero16 : bc1);
    else bc0 = MFMA32(wf[q & 3], hf[q >> 1], q < 2 ? zero16 : bc0);
    SBAR();
    if constexpr (q >= 2 && q < 8) stage_wd(7 + (q - 2) / 2, 7 + (q - 2) / 2, q & 1);
    if constexpr (q >= 16 && q < 24) stage_wd(10 + (q - 16) / 2, (q - 16) / 2, q & 1);
    if constexpr (q == 31) fo = look16(A.ws + F_O + 128 * (rt64 >> 1), wid, lane);
    if constexpr (q >= 34 && q < 41) glds16(wos + (size_t)(8 * (q - 34)) * XROW, ow, lds0 + 65536 + wid * 7168 + (q - 34) * 1024);
    if constexpr (q >= O_STEP && q < O_STEP + 8) of[q - O_STEP] = ldfrag(rs, oo + (q - O_STEP) * 1024);
    SBAR();
  });
  const float gm = rbf(1.f - g);
  lds_cptr wo0 = L + 65536 + kq * (112 * 128) + wro + 32 * ((0 + blk) ^ q4), wo1 = L + 65536 + kq * (112 * 128) + wro + 32 * ((2 + blk) ^ q4);
  lds_cptr wo0h = wo0 + (hh ? 2048 : 3072), wo1h = wo1 + (hh ? 2048 : 3072);
  PIN(wo0); PIN(wo1); PIN(wo0h); PIN(wo1h);
  lds_ptr rp = (lds_ptr)L + (kq < 3 ? kq * (64 * 272) : 122880) + (r3 * 32 + (lane & 31)) * 272 + 16 * hh;
  PIN(rp);
  waitv_bar<8>();
  f32x16 ac0, ac1;
  auto rdO = [&](auto Q) {
    constexpr int q = decltype(Q)::v, b = ((q & 7) >> 1) * 28 * 128;
    if constexpr (q < 16) {
      const lds_cptr w = q < 8 ? wo0 : wo1, wh = q < 8 ? wo0h : wo1h;
      if constexpr (q & 1) wf[q & 3] = cat8(trread(w + b + 2048), trread(wh + b));
      else wf[q & 3] = cat8(trread(w + b), trread(w + b + 1024));
    }
  };
  rdO(IC<0>{}); rdO(IC<1>{}); rdO(IC<2>{});
  sfor<0, 16>([&](auto Q) {
    constexpr int q = decltype(Q)::v;
    rdO(IC<q + 3>{});
    SBAR();
    if constexpr (q < 8) ac0 = MFMA32(wf[q & 3], of[q & 7], q == 0 ? zero16 : ac0);
    else ac1 = MFMA32(wf[q & 3], of[q & 7], q == 8 ? zero16 : ac1);
    SBAR();
    if constexpr (q < 8) {
#pragma unroll
      for (int j = 0; j < 2; ++j) { bc0[2 * q + j] *= gm; bc1[2 * q + j] *= gm; }
      PIN(bc0); PIN(bc1);
    }
    if constexpr (q >= 10 && q < 14) {
      constexpr int i = q - 10;
      *(LAS f32x4*)(rp + 32 * i) = f32x4{g * ac0[4 * i] + bc0[4 * i], g * ac0[4 * i + 1] + bc0[4 * i + 1], g * ac0[4 * i + 2] + bc0[4 * i + 2], g * ac0[4 * i + 3] + bc0[4 * i + 3]};
    }
    SBAR();
  });
#pragma unroll
  for (int i = 0; i < 4; ++i)
    *(LAS f32x4*)(rp + 128 + 32 * i) = f32x4{g * ac1[4 * i] + bc1[4 * i], g * ac1[4 * i + 1] + bc1[4 * i + 1], g * ac1[4 * i + 2] + bc1[4 * i + 2], g * ac1[4 * i + 3] + bc1[4 * i + 3]};
  __syncthreads();
  {
    const int row = tid >> 3, cc = tid & 7;
    const char* rp = lds + row * 272 + cc * 32;
    f32x4 s0 = *(const f32x4*)rp, s1 = *(const f32x4*)(rp + 16);
#pragma unroll
    for (int k = 1; k < 4; ++k) { const int ko = k < 3 ? k * (64 * 272) : 122880; s0 += *(const f32x4*)(rp + ko); s1 += *(const f32x4*)(rp + ko + 16); }
    u32x4 w = {pk2(s0[0], s0[1]), pk2(s0[2], s0[3]), pk2(s1[0], s1[1]), pk2(s1[2], s1[3])};
    const rsrc_t ro = __builtin_amdgcn_make_buffer_rsrc((void*)A.out, 0, 2048 * XROW, 0x00020000);
    st16(ro, (rt64 * 64 + row) * XROW + c7 * 128 + cc * 16, w);
  }
  asm volatile("fused_code_end:");
}

__global__ __launch_bounds__(512, 2) void fused_kernel(Args A) {
  extern __shared__ __attribute__((aligned(16))) char lds[];
  const int bid = blockIdx.x, xcd = bid & 7, idx = bid >> 3;
  const int rb1 = 4 * (xcd & 3) + (idx & 3), cgp = idx < 28 ? 7 * (xcd >> 2) + (idx >> 2) : 14 + (xcd >> 2);
  unsigned cv = 0;
  {
    unsigned long long pc; unsigned len;
    asm volatile("s_getpc_b64 %0\nfused_code_begin:\n\ts_mov_b32 %1, fused_code_end-fused_code_begin\n\ts_max_u32 %1, %1, fused_code_end2-fused_code_begin"
                 : "=s"(pc), "=s"(len)::"scc");
    const unsigned off = (idx * 16 + (threadIdx.x & 15)) * 128;
    if (threadIdx.x < 16 && off < len) asm volatile("global_load_dword %0, %1, %2" : "=v"(cv) : "v"(off), "s"(pc) : "memory");
  }
  {
    const int lane = threadIdx.x & 63, wid = __builtin_amdgcn_readfirstlane(threadIdx.x >> 6);
    const int r8 = lane >> 3, c8 = lane & 7, xr0 = 16 * wid + r8;
    const char* xs = A.x + (size_t)(128 * rb1 + 16 * wid) * XROW + 128 * krot(rb1, cgp);
    const unsigned sb = (unsigned)(uintptr_t)lds + (cgp < 14 ? posA(0) : pos1(0)) + 2048 * wid;
    glds16(xs, r8 * XROW + 16 * (c8 ^ ((xr0 >> 1) & 7)), sb);
    glds16(xs, (r8 + 8) * XROW + 16 * (c8 ^ (((xr0 + 8) >> 1) & 7)), sb + 1024);
  }
  const rsrc_t rs = __builtin_amdgcn_make_buffer_rsrc((void*)A.ws, 0, WS_END, 0x00020000);
  unsigned epoch;
  unsigned tvw = 0;
  KV kv;
  if (__builtin_expect(cgp < 14, 1)) epoch = phase1_gu(A, rs, lds, rb1, cgp, tvw, kv);
  else epoch = phase1_v(A, rs, lds, rb1, cgp, tvw, kv);
  PIN(cv);
  phase23(A, rs, lds, rb1, cgp, epoch, tvw, kv);
}

extern "C" void kernel_launch(void* const* d_in, const int* in_sizes, int n_in, void* d_out, int out_size, void* d_ws, size_t ws_size,
                              hipStream_t stream) {
  Args A;
  A.x = (const char*)d_in[0]; A.wqkv = (const char*)d_in[1]; A.wo = (const char*)d_in[2];
  A.wg = (const char*)d_in[4]; A.wu = (const char*)d_in[5]; A.wd = (const char*)d_in[6]; A.gw = (const char*)d_in[7];
  A.ws = (char*)d_ws; A.out = (char*)d_out;
  (void)hipFuncSetAttribute((const void*)fused_kernel, hipFuncAttributeMaxDynamicSharedMemorySize, LDS_BYTES);
  hipLaunchKernelGGL(fused_kernel, dim3(256), dim3(512), LDS_BYTES, stream, A);
}
```
